```python
import torch

def _softmax(x, axis=-1):
    return torch.softmax(x, dim=axis)

def _gelu(x):
    return torch.nn.functional.gelu(x, approximate='tanh')

def _silu(x):
    return torch.nn.functional.silu(x)

def _sigmoid(x):
    return torch.sigmoid(x)

def _layernorm(x, axis=-1, eps=1e-05):
    m = x.mean(dim=axis, keepdim=True)
    v = x.var(dim=axis, keepdim=True, unbiased=False)
    return (x - m) / torch.sqrt(v + eps)

def _scatter_add(tgt, idx, src, axis=0):
    return tgt.clone().index_add_(axis, idx, src)

def _segment_sum(src, seg, n):
    out = torch.zeros((n,) + src.shape[1:], dtype=src.dtype)
    return out.index_add_(0, seg, src)

def _pad(x, pw):
    flat = tuple((v for pair in reversed(pw) for v in pair))
    return torch.nn.functional.pad(x, flat)
kernel_name = 'sequential_preLN__memory_aug_recurrent_mem_tr__dense_activation_squared_relu__prompt'

def setup_inputs():

    _w = lambda a: (lambda t: t.to(torch.bfloat16) if t.is_floating_point() else t)(torch.as_tensor(a))

    def _A_setup():
        B, S, Dm = (1, 2048, 448)
        H, Dh = (16, 256)
        M, seg = (8, 32)
        seg = min(seg, S)
        S = S // seg * seg
        return {'x': _w((lambda *s: torch.randn(*s, dtype=torch.bfloat16) * 0.05)(B, S, Dm)), 'mem0': _w((lambda *s: torch.randn(*s, dtype=torch.bfloat16) * 0.2)(B, M, Dm)), 'Wq': _w((lambda *s: torch.randn(*s, dtype=torch.bfloat16) * 1.6)(Dm, H, Dh) * Dm ** (-0.5)), 'Wk': _w((lambda *s: torch.randn(*s, dtype=torch.bfloat16) * 1.6)(Dm, H, Dh) * Dm ** (-0.5)), 'Wv': _w((lambda *s: torch.randn(*s, dtype=torch.bfloat16) * 0.2)(Dm, H, Dh) * Dm ** (-0.5)), 'Wo': _w((lambda *s: torch.randn(*s, dtype=torch.bfloat16) * 0.2)(H, Dh, Dm) * (H * Dh) ** (-0.5)), 'seg_len': seg}
    _A_in = _A_setup()

    def _B_setup():
        B, S, D, F = (1, 2048, 448, 1792)
        return {'x': _w((lambda *s: torch.randn(*s, dtype=torch.bfloat16) * 0.05)(B, S, D)), 'w1': _w((lambda *s: torch.randn(*s, dtype=torch.bfloat16) * 0.05)(D, F)), 'b1': _w((lambda *s: torch.randn(*s, dtype=torch.bfloat16) * 0.2)(F)), 'w2': _w((lambda *s: torch.randn(*s, dtype=torch.bfloat16) * 0.05)(F, D)), 'b2': _w((lambda *s: torch.randn(*s, dtype=torch.bfloat16) * 0.8)(D))}
    _B_in = _B_setup()
    return {'A_x': _A_in['x'], 'A_mem0': _A_in['mem0'], 'A_Wq': _A_in['Wq'], 'A_Wk': _A_in['Wk'], 'A_Wv': _A_in['Wv'], 'A_Wo': _A_in['Wo'], 'A_seg_len': _A_in['seg_len'], 'B_x': _B_in['x'], 'B_w1': _B_in['w1'], 'B_b1': _B_in['b1'], 'B_w2': _B_in['w2'], 'B_b2': _B_in['b2']}

def reference(A_x, A_mem0, A_Wq, A_Wk, A_Wv, A_Wo, A_seg_len, B_x, B_w1, B_b1, B_w2, B_b2):

    def _A_ref(x, mem0, Wq, Wk, Wv, Wo, seg_len):
        B, S, Dm = x.shape
        H, Dh = (Wq.shape[1], Wq.shape[2])
        M = mem0.shape[1]
        scale = Dh ** (-0.5)
        n_seg = S // seg_len
        L = M + seg_len
        idx = torch.arange(L)
        mask = (idx[:, None] < M) | (idx[:, None] >= idx[None, :])
        mem = mem0
        outs = []
        for s in range(n_seg):
            xs = x[:, s * seg_len:(s + 1) * seg_len, :].clone()
            h = torch.concatenate([mem, xs], axis=1)
            q = torch.einsum('btd,dhe->bhte', h, Wq)
            k = torch.einsum('btd,dhe->bhte', h, Wk)
            v = torch.einsum('btd,dhe->bhte', h, Wv)
            scores = torch.einsum('bhqe,bhke->bhqk', q, k) * scale
            scores = torch.where(mask[None, None], scores, -1000000000.0)
            p = _softmax(scores, axis=-1)
            o = torch.einsum('bhqk,bhke->bhqe', p, v)
            o = torch.einsum('bhte,hed->btd', o, Wo)
            mem = o[:, :M, :].clone()
            outs.append(o[:, M:, :])
        return torch.concatenate(outs, axis=1)

    def _B_ref(x, w1, b1, w2, b2):
        h = torch.einsum('bsd,df->bsf', x, w1) + b1
        zero = torch.zeros_like(h)
        r = torch.where(h > zero, h, zero)
        h = r * r
        out = torch.einsum('bsf,fd->bsd', h, w2) + b2
        return out
    x = A_x
    h = x + _A_ref(_layernorm(x, axis=-1), A_mem0, A_Wq, A_Wk, A_Wv, A_Wo, A_seg_len)
    h2 = h + _B_ref(_layernorm(h, axis=-1), B_w1, B_b1, B_w2, B_b2)
    return h2
```

```cpp
#include <hip/hip_runtime.h>
#include <hip/hip_bf16.h>
#include <stdint.h>

#define LAS __attribute__((address_space(3)))
#define GAS __attribute__((address_space(1)))
typedef __attribute__((ext_vector_type(2))) __bf16 bf16x2;
typedef __attribute__((ext_vector_type(4))) __bf16 bf16x4;
typedef __attribute__((ext_vector_type(8))) __bf16 bf16x8;
typedef __attribute__((ext_vector_type(16))) __bf16 bf16x16;
typedef __attribute__((ext_vector_type(4))) short s16x4;
typedef __attribute__((ext_vector_type(4))) float f32x4;
typedef __attribute__((ext_vector_type(2))) float f32x2;
typedef unsigned v4u __attribute__((ext_vector_type(4)));
typedef unsigned v2u __attribute__((ext_vector_type(2)));
typedef LAS char* lptr;

constexpr int D = 448, H = 16, E = 256, NM = 8, L = 32, NSEG = 64, T = 2048, F = 1792;
constexpr unsigned SENT = 0xFFFFFFFFu;

constexpr size_t OFF_CNT = 0;
constexpr int CNT_VW = 0, CNT_PX = 1, CNT_H = 2, CNT_HID = 3;
constexpr size_t OFF_ERR = 32768;
constexpr size_t OFF_DBG = 36864;
constexpr size_t OFF_DBG2 = 49152;
constexpr size_t OFF_DBG3 = 196608;
constexpr size_t OFF_WM = 1u << 20;
constexpr size_t OFF_WPX = 2u << 20;
constexpr size_t OFF_WPM = 3u << 20;
constexpr size_t WSTR = 16384;
constexpr unsigned CAL_REL = (2u << 20) + 63 * 16384 + 6144;
constexpr size_t OFF_MH2 = 512u << 10;
constexpr size_t OFF_XN = 4u << 20;
constexpr size_t OFF_AT = OFF_XN + (size_t)T * D * 2;
constexpr size_t OFF_BT = OFF_AT + (size_t)H * D * D * 2;
constexpr size_t OFF_BS = OFF_BT + (size_t)H * D * D * 2;
constexpr size_t OFF_VW = OFF_BS + (size_t)H * D * D * 2;
constexpr size_t VW_SEG = 16 * 2 * 16 * 1024;
constexpr size_t OFF_PX = OFF_VW + 64 * VW_SEG;
constexpr size_t PX_SEG = 40960;
constexpr size_t OFF_HB = OFF_PX + 64 * PX_SEG;
constexpr size_t OFF_HID = OFF_HB + (size_t)T * D * 2;
constexpr size_t WS_END = OFF_HID + (size_t)T * F * 2;

constexpr int DIRECT0 = 60;
constexpr float LOG2E = 1.4426950408889634f;

struct Params {
    const unsigned short* x; const unsigned short* mem0; const unsigned short* Wq; const unsigned short* Wk;
    const unsigned short* Wv; const unsigned short* Wo; const unsigned short* w1; const unsigned short* b1;
    const unsigned short* w2; const unsigned short* b2; unsigned short* out; char* ws;
    int par; int dS; int dM; int dP; int fast; int slow; int up; int hold;
};

__device__ __forceinline__ float bf2f(unsigned short u) { return __builtin_bit_cast(float, (unsigned)u << 16); }
__device__ __forceinline__ float bflo(unsigned u) { return __builtin_bit_cast(float, u << 16); }
__device__ __forceinline__ float bfhi(unsigned u) { return __builtin_bit_cast(float, u & 0xffff0000u); }
__device__ __forceinline__ unsigned pk2(float a, float b) { bf16x2 p = {(__bf16)a, (__bf16)b}; return __builtin_bit_cast(unsigned, p); }
__device__ __forceinline__ float asf(unsigned x) { return __builtin_bit_cast(float, x); }
__device__ __forceinline__ unsigned asu(float x) { return __builtin_bit_cast(unsigned, x); }
__device__ __forceinline__ bf16x8 as8(v4u v) { return __builtin_bit_cast(bf16x8, v); }
__device__ __forceinline__ bf16x8 cat4(bf16x4 a, bf16x4 b) { return __builtin_shufflevector(a, b, 0, 1, 2, 3, 4, 5, 6, 7); }
__device__ __forceinline__ bf16x16 cat8(bf16x8 a, bf16x8 b) { return __builtin_shufflevector(a, b, 0, 1, 2, 3, 4, 5, 6, 7, 8, 9, 10, 11, 12, 13, 14, 15); }
__device__ __forceinline__ f32x4 mfma32(bf16x8 a, bf16x8 b, f32x4 c) { return __builtin_amdgcn_mfma_f32_16x16x32_bf16(a, b, c, 0, 0, 0); }
__device__ __forceinline__ f32x4 mfma16(v2u a, v2u b, f32x4 c) { return __builtin_amdgcn_mfma_f32_16x16x16bf16_1k(__builtin_bit_cast(s16x4, a), __builtin_bit_cast(s16x4, b), c, 0, 0, 0); }
__device__ __forceinline__ f32x4 smf(bf16x8 a, bf16x16 b, f32x4 c, int idx) { return __builtin_amdgcn_smfmac_f32_16x16x64_bf16(a, b, c, idx, 0, 0); }
__device__ __forceinline__ v4u lds_r128(lptr p) { return *(LAS v4u*)p; }
__device__ __forceinline__ v2u lds_r64(lptr p) { return *(LAS v2u*)p; }
__device__ __forceinline__ bf16x4 lds_tr(lptr p) { return __builtin_amdgcn_ds_read_tr16_b64_v4bf16((LAS bf16x4*)p); }
__device__ __forceinline__ v2u lds_tru(lptr p) { return __builtin_bit_cast(v2u, __builtin_amdgcn_ds_read_tr16_b64_v4bf16((LAS bf16x4*)p)); }
__device__ __forceinline__ unsigned san(unsigned x) { return x < 0xFFFFFFFEu ? x : 0xFFFFFFFEu; }

__device__ __forceinline__ unsigned umax(unsigned a, unsigned b) { return a > b ? a : b; }
__device__ __forceinline__ unsigned mx4(unsigned m, v4u v) { return umax(umax(umax(m, v.x), v.y), umax(v.z, v.w)); }
__device__ __forceinline__ bool anyS(unsigned m) { return __builtin_amdgcn_ballot_w64(m == SENT) != 0; }
typedef __amdgpu_buffer_rsrc_t rsrc_t;
__device__ __forceinline__ rsrc_t mkrs(const void* p) { return __builtin_amdgcn_make_buffer_rsrc((void*)p, 0, 0x7fffffff, 0x00020000); }
__device__ __forceinline__ v4u gld128(rsrc_t r, unsigned off) { return __builtin_amdgcn_raw_buffer_load_b128(r, off, 0, 16); }
__device__ __forceinline__ v2u gld64(rsrc_t r, unsigned off) { return __builtin_amdgcn_raw_buffer_load_b64(r, off, 0, 16); }
__device__ __forceinline__ v4u gld128p(rsrc_t r, unsigned off) { return __builtin_amdgcn_raw_buffer_load_b128(r, off, 0, 0); }
__device__ __forceinline__ void gst128(rsrc_t r, unsigned off, v4u v) { __builtin_amdgcn_raw_buffer_store_b128(v, r, off, 0, 16); }
__device__ __forceinline__ void gst64(rsrc_t r, unsigned off, v2u v) { __builtin_amdgcn_raw_buffer_store_b64(v, r, off, 0, 16); }
__device__ __forceinline__ void gst32(rsrc_t r, unsigned off, unsigned v) { __builtin_amdgcn_raw_buffer_store_b32(v, r, off, 0, 16); }
constexpr unsigned OOB = 0x80000000u;

#define RLX_AGENT __ATOMIC_RELAXED, __HIP_MEMORY_SCOPE_AGENT
typedef GAS unsigned gu32;
constexpr unsigned SPIN_CAP = 1u << 16;
__device__ __forceinline__ gu32* cntp(char* ws, int which, int s) { return (gu32*)(ws + OFF_CNT + ((size_t)which * 64 + s) * 128); }
__device__ __forceinline__ void set_err(char* ws, unsigned code) { __hip_atomic_store((gu32*)(ws + OFF_ERR), code, RLX_AGENT); }
__device__ __forceinline__ bool has_err(char* ws) { return __builtin_amdgcn_readfirstlane(__hip_atomic_load((gu32*)(ws + OFF_ERR), RLX_AGENT)) != 0; }
__device__ __forceinline__ void wait_cnt(char* ws, int which, int s, unsigned target, unsigned code) {
    gu32* p = cntp(ws, which, s);
    for (unsigned sp = 0;; ++sp) {
        asm volatile("" ::: "memory");
        unsigned v = __builtin_amdgcn_readfirstlane(__hip_atomic_load(p, RLX_AGENT));
        if (v >= target) break;
        __builtin_amdgcn_s_sleep(2);
        if ((sp & 63u) == 63u && has_err(ws)) break;
        if (sp > SPIN_CAP) { set_err(ws, code); break; }
    }
}
__device__ __forceinline__ void signal_cnt(char* ws, int which, int s) {
    asm volatile("s_waitcnt vmcnt(0)" ::: "memory");
    __syncthreads();
    if (threadIdx.x == 0) __hip_atomic_fetch_add(cntp(ws, which, s), 1u, RLX_AGENT);
}

constexpr int K0_RS = 528;
constexpr int K0_WSTRIDE = 232;
template <bool isB>
__device__ __forceinline__ void k0_body(const Params& p, lptr smem) {
    const int bid = blockIdx.x, tid = threadIdx.x, l = tid & 63, w = __builtin_amdgcn_readfirstlane(tid >> 6);
    const int g = l >> 4, n = l & 15;
    const int mat = (bid & 7) * 4 + (bid >> 6), sub = (bid >> 3) & 7, mch = sub & 3, ng = sub >> 2;
    const int h = mat & 15;
    const int m0 = 112 * mch;
    v4u sm_[14], sn_[32];
    const unsigned short* Q = (isB ? p.Wv : p.Wq) + h * 256;
    const int nb = 256 * ng;
    if constexpr (!isB) {
        const unsigned short* src = p.Wk + (size_t)m0 * 4096 + h * 256;
#pragma unroll
        for (int c = 0; c < 14; ++c) { const int q = c * 256 + tid; sm_[c] = *(const v4u*)(src + (size_t)(q >> 5) * 4096 + (q & 31) * 8); }
    } else {
        const unsigned short* src = p.Wo + (size_t)h * 256 * 448 + m0;
#pragma unroll
        for (int c = 0; c < 14; ++c) { const int q = c * 256 + tid; sm_[c] = *(const v4u*)(src + (size_t)(q / 14) * 448 + (q % 14) * 8); }
    }
#pragma unroll
    for (int c = 0; c < 32; ++c) { const int q = c * 256 + tid; int row = nb + (q >> 5); row = row < 448 ? row : 447; sn_[c] = *(const v4u*)(Q + (size_t)row * 4096 + (q & 31) * 8); }
    v4u lv[7];
    const int lrow = bid * 8 + (l >> 3), lsub = l & 7;
    if (w == 3) {
#pragma unroll
        for (int c = 0; c < 7; ++c) lv[c] = *(const v4u*)(p.x + (size_t)lrow * D + (c * 8 + lsub) * 8);
    }
    rsrc_t rs = mkrs(p.ws);
    {
#pragma unroll
        for (int c = 0; c < 3; ++c) { const unsigned o = ((unsigned)bid * 3 + c) * 4096 + tid * 16; gst128(rs, (o >= WSTR && (o < CAL_REL || o >= CAL_REL + 1024)) ? (unsigned)OFF_WM + o : OOB, v4u{SENT, SENT, SENT, SENT}); }
        if (bid >= 4) { const size_t o = (size_t)bid * 2048 + tid * 8; *(v2u*)(p.ws + OFF_MH2 + o) = v2u{SENT, SENT}; }
        {
            constexpr unsigned n0 = (NSEG - DIRECT0) * PX_SEG / 16, n1 = (NSEG - DIRECT0) * L * D * 2 / 16, n2 = (NSEG - DIRECT0) * L * F * 2 / 16;
            const unsigned q = bid * 256 + tid;
            if (q < n0) *(v4u*)(p.ws + OFF_PX + (size_t)DIRECT0 * PX_SEG + (size_t)q * 16) = v4u{SENT, SENT, SENT, SENT};
            if (q < n1) *(v4u*)(p.ws + OFF_HB + (size_t)DIRECT0 * L * D * 2 + (size_t)q * 16) = v4u{SENT, SENT, SENT, SENT};
            if (q < n2) *(v4u*)(p.ws + OFF_HID + (size_t)DIRECT0 * L * F * 2 + (size_t)q * 16) = v4u{SENT, SENT, SENT, SENT};
        }
        if (bid < 9) *(v4u*)(p.ws + OFF_CNT + (size_t)(bid * 256 + tid) * 16) = v4u{0, 0, 0, 0};
        if (bid >= 16 && bid < 24) {
            const int c = (bid - 16) * 256 + tid; const int j = c >> 7, P = (c >> 3) & 15, row = c & 7; const int c0 = 4 * (P >> 1) + (P & 1);
            unsigned lo = 0, hi = 0; if (P < 14) { lo = p.mem0[row * D + 28 * j + c0]; hi = p.mem0[row * D + 28 * j + c0 + 2]; }
            const unsigned o = san(lo | (hi << 16));
            *(unsigned*)(p.ws + OFF_WM + (size_t)c * 4) = o; *(unsigned*)(p.ws + OFF_WM + 8192 + (size_t)c * 4) = o; *(unsigned*)(p.ws + OFF_MH2 + (size_t)c * 4) = o;
        }
    }
    constexpr int NOFF = 60416;
    if constexpr (!isB) {
#pragma unroll
        for (int c = 0; c < 14; ++c) { const int q = c * 256 + tid; *(LAS v4u*)(smem + (q >> 5) * K0_RS + (q & 31) * 16) = sm_[c]; }
    } else {
#pragma unroll
        for (int c = 0; c < 14; ++c) { const int q = c * 256 + tid; lptr d = smem + (q / 14) * K0_WSTRIDE + (q % 14) * 16; *(LAS v2u*)d = v2u{sm_[c].x, sm_[c].y}; *(LAS v2u*)(d + 8) = v2u{sm_[c].z, sm_[c].w}; }
    }
#pragma unroll
    for (int ps = 0; ps < 2; ++ps) {
        if (ps) __syncthreads();
#pragma unroll
        for (int c = 0; c < 16; ++c) { const int q = c * 256 + tid; *(LAS v4u*)(smem + NOFF + (q >> 5) * K0_RS + (q & 31) * 16) = sn_[16 * ps + c]; }
        __syncthreads();
        const int n0 = nb + 128 * ps + 32 * w;
        if (ps == 1 && w == 2 && (bid & 7) == 0 && ng == 1 && (bid >> 3) < 16 && (((bid >> 3) & 3) + 4 * (bid >> 6)) < 6) {
            const int q = ((bid >> 3) & 3) + 4 * (bid >> 6);
            const unsigned base = (unsigned)(OFF_WM + (size_t)(q >> 1) * (1u << 20) + 62 * WSTR + (q & 1) * 8192);
            unsigned best = 0x7fffffffu;
            for (int r = 0; r < 4; ++r) {
                unsigned long long t0, t1; unsigned v;
                asm volatile("s_waitcnt vmcnt(0)\n\ts_memtime %0\n\ts_waitcnt lgkmcnt(0)" : "=s"(t0) :: "memory");
                v = __builtin_amdgcn_raw_buffer_load_b32(rs, base + r * 1024, 0, 16);
                asm volatile("s_waitcnt vmcnt(0)\n\ts_memtime %0\n\ts_waitcnt lgkmcnt(0)" : "=s"(t1) : "v"(v) : "memory");
                const unsigned d = (unsigned)(t1 - t0);
                best = d < best ? d : best;
            }
            gst32(rs, l == 0 ? (unsigned)OFF_WM + CAL_REL + q * 128 : OOB, best);
        }
        if (n0 < 448) {
            f32x4 acc[2][7];
#pragma unroll
            for (int kk = 0; kk < 8; ++kk) {
                bf16x8 af[2], bfr[7];
#pragma unroll
                for (int a = 0; a < 2; ++a) af[a] = as8(lds_r128(smem + NOFF + (32 * w + 8 * (n >> 2) + 4 * a + (n & 3)) * K0_RS + (32 * kk + 8 * g) * 2));
#pragma unroll
                for (int t = 0; t < 7; ++t) {
                    if constexpr (!isB) bfr[t] = as8(lds_r128(smem + (16 * t + n) * K0_RS + (32 * kk + 8 * g) * 2));
                    else { lptr a0 = smem + (32 * kk + 8 * g + (n >> 2)) * K0_WSTRIDE + (16 * t + 4 * (n & 3)) * 2; bfr[t] = cat4(lds_tr(a0), lds_tr(a0 + 4 * K0_WSTRIDE)); }
                }
#pragma unroll
                for (int t = 0; t < 7; ++t)
#pragma unroll
                    for (int a = 0; a < 2; ++a) acc[a][t] = mfma32(af[a], bfr[t], kk == 0 ? f32x4{0, 0, 0, 0} : acc[a][t]);
            }
            const float sc = isB ? 1.0f : (0.0625f * LOG2E);
            const int a0c = n0 + 8 * g, xq = (a0c >> 3) & 3;
            const unsigned colpart = ((a0c >> 5) * 64 + (((xq & 1) << 1) | (xq >> 1)) * 16) * 16;
#pragma unroll
            for (int t = 0; t < 7; ++t) {
                const f32x4 a0 = acc[0][t] * sc, a1 = acc[1][t] * sc;
                const v4u val = {pk2(a0[0], a0[1]), pk2(a0[2], a0[3]), pk2(a1[0], a1[1]), pk2(a1[2], a1[3])};
                const int r = m0 + 16 * t + n;
                gst128(rs, (unsigned)((isB ? OFF_BT : OFF_AT) + (size_t)h * D * D * 2) + ((((r >> 4) & 3) * 7 + (r >> 6)) * 14 * 64 + (r & 15)) * 16 + colpart, val);
                if constexpr (isB) { const int jj = r / 28, v = 28 * h + r % 28;
                    gst128(rs, (unsigned)(OFF_BS + (size_t)jj * D * D * 2) + ((((v >> 4) & 3) * 7 + (v >> 6)) * 14 * 64 + (v & 15)) * 16 + colpart, val); }
            }
        }
    }
    if (w == 3) {
        float s = 0.f;
#pragma unroll
        for (int c = 0; c < 7; ++c)
#pragma unroll
            for (int e = 0; e < 4; ++e) s += bflo(lv[c][e]) + bfhi(lv[c][e]);
        s += __shfl_xor(s, 1); s += __shfl_xor(s, 2); s += __shfl_xor(s, 4);
        const float mean = s * (1.0f / D);
        float q = 0.f;
#pragma unroll
        for (int c = 0; c < 7; ++c)
#pragma unroll
            for (int e = 0; e < 4; ++e) { float a = bflo(lv[c][e]) - mean, b = bfhi(lv[c][e]) - mean; q += a * a + b * b; }
        q += __shfl_xor(q, 1); q += __shfl_xor(q, 2); q += __shfl_xor(q, 4);
        const float rsd = 1.0f / sqrtf(q * (1.0f / D) + 1e-5f);
#pragma unroll
        for (int c = 0; c < 7; ++c) { v4u o;
#pragma unroll
            for (int e = 0; e < 4; ++e) o[e] = pk2((bflo(lv[c][e]) - mean) * rsd, (bfhi(lv[c][e]) - mean) * rsd);
            gst128(rs, (unsigned)(OFF_XN + ((size_t)lrow * D + (c * 8 + lsub) * 8) * 2), o); }
    }
}

__global__ void __launch_bounds__(256, 1) k0(Params p) {
    extern __shared__ __attribute__((aligned(16))) char smem_[];
    if ((blockIdx.x & 7) >= 4) k0_body<true>(p, (lptr)smem_); else k0_body<false>(p, (lptr)smem_);
}

constexpr int XSTR = 912;
constexpr int XIMG_BYTES = 32 * XSTR;
template <bool SC>
__device__ __forceinline__ void stage_rows(rsrc_t rs, unsigned rowbase_bytes, lptr img, int tid) {
    v4u v[7];
#pragma unroll
    for (int c = 0; c < 7; ++c) { int q = c * 256 + tid; unsigned off = rowbase_bytes + q * 16; v[c] = SC ? gld128(rs, off) : gld128p(rs, off); }
#pragma unroll
    for (int c = 0; c < 7; ++c) { int q = c * 256 + tid; int r = q / 56, ch = q % 56; *(LAS v4u*)(img + r * XSTR + ch * 16) = v[c]; }
}
__device__ __forceinline__ bool m_bad(v4u a, v4u b) { return anyS(mx4(mx4(0u, a), b)); }
__device__ __forceinline__ void gather_m(char* ws, rsrc_t rs, unsigned slot, lptr mt, lptr dummy, int w, int l, unsigned code, v4u a, v4u b) {
    const unsigned base = slot + w * 2048 + l * 16;
    for (unsigned sp = 0; m_bad(a, b); ++sp) {
        __builtin_amdgcn_s_sleep(2);
        asm volatile("" ::: "memory");
        a = gld128(rs, base); b = gld128(rs, base + 1024);
        if ((sp & 63u) == 63u && has_err(ws)) break;
        if (sp > SPIN_CAP) { set_err(ws, code); break; }
    }
    { int q = w * 128 + l; int j = q >> 5, c = q & 31; *(LAS v4u*)(c < 28 ? mt + (28 * j + c) * 16 : dummy + l * 16) = a; }
    { int q = w * 128 + 64 + l; int j = q >> 5, c = q & 31; *(LAS v4u*)(c < 28 ? mt + (28 * j + c) * 16 : dummy + l * 16) = b; }
}
__device__ __forceinline__ int afrag_off(int l) {
    const int ga = l >> 4, q = (l >> 2) & 3, pp = l & 3;
    const int pi = 16 * (ga >> 1) + 4 * (ga & 1) + q;
    return pi * 32 + 8 * pp;
}
__device__ __forceinline__ bf16x8 afrag(lptr img, int aoff, int kk) { lptr a = img + aoff + kk * 1024; return cat4(lds_tr(a), lds_tr(a + 256)); }

__device__ __forceinline__ float dpp_max8(float v) {
    v = fmaxf(v, asf(__builtin_amdgcn_update_dpp(0, (int)asu(v), 0xB1, 0xf, 0xf, true)));
    v = fmaxf(v, asf(__builtin_amdgcn_update_dpp(0, (int)asu(v), 0x4E, 0xf, 0xf, true)));
    v = fmaxf(v, asf(__builtin_amdgcn_update_dpp(0, (int)asu(v), 0x141, 0xf, 0xf, true)));
    return v;
}
__device__ __forceinline__ float dpp_sum8(float v) {
    v += asf(__builtin_amdgcn_update_dpp(0, (int)asu(v), 0xB1, 0xf, 0xf, true));
    v += asf(__builtin_amdgcn_update_dpp(0, (int)asu(v), 0x4E, 0xf, 0xf, true));
    v += asf(__builtin_amdgcn_update_dpp(0, (int)asu(v), 0x141, 0xf, 0xf, true));
    return v;
}
__device__ __forceinline__ float dpp_xor1(float v) { return asf(__builtin_amdgcn_update_dpp(0, (int)asu(v), 0xB1, 0xf, 0xf, true)); }
__device__ __forceinline__ int sigma(int i) { return (i & 4) | ((i & 1) << 1) | ((i >> 1) & 1); }

__device__ __forceinline__ void halfsum(f32x4 d, float& y01, float& y23) {
    auto r0 = __builtin_amdgcn_permlane32_swap(asu(d[0]), asu(d[1]), false, false);
    auto r1 = __builtin_amdgcn_permlane32_swap(asu(d[2]), asu(d[3]), false, false);
    y01 = asf(r0[0]) + asf(r0[1]); y23 = asf(r1[0]) + asf(r1[1]);
}

#define LOAD_SPARSE_W(wt, rs, base)                                                                      \
    _Pragma("unroll") for (int j = 0; j < 7; ++j) _Pragma("unroll") for (int k = 0; k < 7; ++k) {        \
        const unsigned o = (base) + ((w * 7 + j) * 14 + 2 * k) * 1024 + l * 16;                           \
        wt[j][k] = cat8(as8(gld128p(rs, o)), as8(gld128p(rs, o + 1024))); }

constexpr int XI_BYTES = 32 * 896;
constexpr int S_MP = 2 * XI_BYTES, S_TP = S_MP + 7168, S_SX = S_TP + 7168, S_DUM = S_SX + 64 * 40;
__device__ __forceinline__ int xswz(int row) { const int v = (row >> 1) & 7; return v < 2 ? v + 4 : (v < 6 ? v - 2 : v); }
__device__ __forceinline__ int opaque_lane() { int l; asm volatile("v_mbcnt_lo_u32_b32 %0, -1, 0\n\tv_mbcnt_hi_u32_b32 %0, -1, %0" : "=v"(l)); return l; }
__device__ __forceinline__ void role_S(const Params& p, int h, int w) {
    const int l = opaque_lane();
    const int g = l >> 4, n = l & 15;
    const unsigned idx = (l & 1) ? 0xEEEE : 0x4444;
    const unsigned a2 = S_MP + afrag_off(l);
    const unsigned a3 = w * 2048 + l * 16;
    unsigned a4, a5;
    { int q = w * 128 + l; int j = q >> 5, c = q & 31; a4 = c < 28 ? S_MP + (28 * j + c) * 16 : S_DUM + l * 16; }
    { int q = w * 128 + 64 + l; int j = q >> 5, c = q & 31; a5 = c < 28 ? S_MP + (28 * j + c) * 16 : S_DUM + l * 16; }
    const int col = 16 * w + n;
    const unsigned a6 = S_TP + (col >> 2) * 64 + (col & 1) * 32 + (2 * g) * 4 + ((col >> 1) & 1) * 2;
    unsigned a7, a0, a8;
    if (w < 2) { const int row = 16 * w + n, x1 = 2 * (g & 1) + (g >> 1), sw = xswz(row);
        a7 = row * 896 + 16 * (x1 ^ sw); a0 = row * 896 + 16 * ((x1 + 4) ^ sw);
        a8 = S_SX + (16 * w + n) * 40 + (2 * g) * 4; }
    else { const int Qp = 4 * (g & 1) + 2 * (g >> 1);
        a7 = S_MP + (2 * Qp + ((n >> 2) & 1)) * 32 + 8 * (n & 3); a0 = 0;
        a8 = (n & 1) ? S_DUM + l * 8 : S_SX + (32 + 8 * (w - 2) + (n >> 1)) * 40 + (2 * g) * 4; }
    const int i = l >> 3, q8 = l & 7, b = q8 >> 2, half = (q8 >> 1) & 1, qh = q8 & 1;
    const unsigned a9 = S_SX + (8 * b + 4 * qh + 2 * half) * 40 + i * 4;
    const unsigned a10 = S_SX + (32 + q8) * 40 + i * 4, a20 = a10 + 8 * 40;
    const unsigned a11 = h * 512 + b * 256 + i * 32 + half * 16 + qh * 8;
    const unsigned a12 = (q8 & 1) ? OOB : h * 128 + i * 16 + (q8 >> 1) * 4;
    unsigned xa[7];
#pragma unroll
    for (int c = 0; c < 7; ++c) { const int q = (7 * w + c) * 64 + l, row = q / 56, chp = q % 56, ch = (chp & ~7) | ((chp ^ xswz(row)) & 7); xa[c] = (row * 56 + ch) * 16; }
    unsigned a21 = l * 16;
    const unsigned a22 = l == 0 ? 0u : OOB;
    const unsigned long long wsa = (unsigned long long)p.ws;
    asm volatile(R"ASM(
s_mov_b32 s50, s45
buffer_load_dwordx4 a[0:3], v21, s[40:43], s50 offen offset:0
buffer_load_dwordx4 a[4:7], v21, s[40:43], s50 offen offset:1024
buffer_load_dwordx4 a[8:11], v21, s[40:43], s50 offen offset:2048
buffer_load_dwordx4 a[12:15], v21, s[40:43], s50 offen offset:3072
s_add_u32 s50, s50, 4096
buffer_load_dwordx4 a[16:19], v21, s[40:43], s50 offen offset:0
buffer_load_dwordx4 a[20:23], v21, s[40:43], s50 offen offset:1024
buffer_load_dwordx4 a[24:27], v21, s[40:43], s50 offen offset:2048
buffer_load_dwordx4 a[28:31], v21, s[40:43], s50 offen offset:3072
s_add_u32 s50, s50, 4096
buffer_load_dwordx4 a[32:35], v21, s[40:43], s50 offen offset:0
buffer_load_dwordx4 a[36:39], v21, s[40:43], s50 offen offset:1024
buffer_load_dwordx4 a[40:43], v21, s[40:43], s50 offen offset:2048
buffer_load_dwordx4 a[44:47], v21, s[40:43], s50 offen offset:3072
s_add_u32 s50, s50, 4096
buffer_load_dwordx4 a[48:51], v21, s[40:43], s50 offen offset:0
buffer_load_dwordx4 a[52:55], v21, s[40:43], s50 offen offset:1024
buffer_load_dwordx4 a[56:59], v21, s[40:43], s50 offen offset:2048
buffer_load_dwordx4 a[60:63], v21, s[40:43], s50 offen offset:3072
s_add_u32 s50, s50, 4096
buffer_load_dwordx4 a[64:67], v21, s[40:43], s50 offen offset:0
buffer_load_dwordx4 a[68:71], v21, s[40:43], s50 offen offset:1024
buffer_load_dwordx4 a[72:75], v21, s[40:43], s50 offen offset:2048
buffer_load_dwordx4 a[76:79], v21, s[40:43], s50 offen offset:3072
s_add_u32 s50, s50, 4096
buffer_load_dwordx4 a[80:83], v21, s[40:43], s50 offen offset:0
buffer_load_dwordx4 a[84:87], v21, s[40:43], s50 offen offset:1024
buffer_load_dwordx4 a[88:91], v21, s[40:43], s50 offen offset:2048
buffer_load_dwordx4 a[92:95], v21, s[40:43], s50 offen offset:3072
s_add_u32 s50, s50, 4096
buffer_load_dwordx4 a[96:99], v21, s[40:43], s50 offen offset:0
buffer_load_dwordx4 a[100:103], v21, s[40:43], s50 offen offset:1024
buffer_load_dwordx4 a[104:107], v21, s[40:43], s50 offen offset:2048
buffer_load_dwordx4 a[108:111], v21, s[40:43], s50 offen offset:3072
s_add_u32 s50, s50, 4096
buffer_load_dwordx4 a[112:115], v21, s[40:43], s50 offen offset:0
buffer_load_dwordx4 a[116:119], v21, s[40:43], s50 offen offset:1024
buffer_load_dwordx4 a[120:123], v21, s[40:43], s50 offen offset:2048
buffer_load_dwordx4 a[124:127], v21, s[40:43], s50 offen offset:3072
s_add_u32 s50, s50, 4096
buffer_load_dwordx4 a[128:131], v21, s[40:43], s50 offen offset:0
buffer_load_dwordx4 a[132:135], v21, s[40:43], s50 offen offset:1024
buffer_load_dwordx4 a[136:139], v21, s[40:43], s50 offen offset:2048
buffer_load_dwordx4 a[140:143], v21, s[40:43], s50 offen offset:3072
s_add_u32 s50, s50, 4096
buffer_load_dwordx4 a[144:147], v21, s[40:43], s50 offen offset:0
buffer_load_dwordx4 a[148:151], v21, s[40:43], s50 offen offset:1024
buffer_load_dwordx4 a[152:155], v21, s[40:43], s50 offen offset:2048
buffer_load_dwordx4 a[156:159], v21, s[40:43], s50 offen offset:3072
s_add_u32 s50, s50, 4096
buffer_load_dwordx4 a[160:163], v21, s[40:43], s50 offen offset:0
buffer_load_dwordx4 a[164:167], v21, s[40:43], s50 offen offset:1024
s_waitcnt vmcnt(28)
buffer_load_dwordx4 a[168:171], v21, s[40:43], s50 offen offset:2048
buffer_load_dwordx4 a[172:175], v21, s[40:43], s50 offen offset:3072
s_add_u32 s50, s50, 4096
buffer_load_dwordx4 a[176:179], v21, s[40:43], s50 offen offset:0
buffer_load_dwordx4 a[180:183], v21, s[40:43], s50 offen offset:1024
buffer_load_dwordx4 a[184:187], v21, s[40:43], s50 offen offset:2048
buffer_load_dwordx4 a[188:191], v21, s[40:43], s50 offen offset:3072
s_add_u32 s50, s50, 4096
buffer_load_dwordx4 a[192:195], v21, s[40:43], s50 offen offset:0
buffer_load_dwordx4 a[196:199], v21, s[40:43], s50 offen offset:1024
buffer_load_dwordx4 a[200:203], v21, s[40:43], s50 offen offset:2048
buffer_load_dwordx4 a[204:207], v21, s[40:43], s50 offen offset:3072
s_add_u32 s50, s50, 4096
buffer_load_dwordx4 a[208:211], v21, s[40:43], s50 offen offset:0
buffer_load_dwordx4 a[212:215], v21, s[40:43], s50 offen offset:1024
buffer_load_dwordx4 a[216:219], v21, s[40:43], s50 offen offset:2048
buffer_load_dwordx4 a[220:223], v21, s[40:43], s50 offen offset:3072
s_add_u32 s50, s50, 4096
s_waitcnt vmcnt(28)
buffer_load_dwordx4 a[224:227], v21, s[40:43], s50 offen offset:0
buffer_load_dwordx4 a[228:231], v21, s[40:43], s50 offen offset:1024
buffer_load_dwordx4 a[232:235], v21, s[40:43], s50 offen offset:2048
buffer_load_dwordx4 a[236:239], v21, s[40:43], s50 offen offset:3072
s_add_u32 s50, s50, 4096
buffer_load_dwordx4 a[240:243], v21, s[40:43], s50 offen offset:0
buffer_load_dwordx4 a[244:247], v21, s[40:43], s50 offen offset:1024
buffer_load_dwordx4 a[248:251], v21, s[40:43], s50 offen offset:2048
buffer_load_dwordx4 a[252:255], v21, s[40:43], s50 offen offset:3072
s_add_u32 s50, s50, 4096
buffer_load_dwordx4 v[120:123], v21, s[40:43], s50 offen offset:0
buffer_load_dwordx4 v[124:127], v21, s[40:43], s50 offen offset:1024
buffer_load_dwordx4 v[128:131], v21, s[40:43], s50 offen offset:2048
buffer_load_dwordx4 v[132:135], v21, s[40:43], s50 offen offset:3072
s_add_u32 s50, s50, 4096
buffer_load_dwordx4 v[136:139], v21, s[40:43], s50 offen offset:0
buffer_load_dwordx4 v[140:143], v21, s[40:43], s50 offen offset:1024
s_waitcnt vmcnt(28)
buffer_load_dwordx4 v[144:147], v21, s[40:43], s50 offen offset:2048
buffer_load_dwordx4 v[148:151], v21, s[40:43], s50 offen offset:3072
s_add_u32 s50, s50, 4096
buffer_load_dwordx4 v[152:155], v21, s[40:43], s50 offen offset:0
buffer_load_dwordx4 v[156:159], v21, s[40:43], s50 offen offset:1024
buffer_load_dwordx4 v[160:163], v21, s[40:43], s50 offen offset:2048
buffer_load_dwordx4 v[164:167], v21, s[40:43], s50 offen offset:3072
s_add_u32 s50, s50, 4096
buffer_load_dwordx4 v[168:171], v21, s[40:43], s50 offen offset:0
buffer_load_dwordx4 v[172:175], v21, s[40:43], s50 offen offset:1024
buffer_load_dwordx4 v[176:179], v21, s[40:43], s50 offen offset:2048
buffer_load_dwordx4 v[180:183], v21, s[40:43], s50 offen offset:3072
s_add_u32 s50, s50, 4096
buffer_load_dwordx4 v[184:187], v21, s[40:43], s50 offen offset:0
buffer_load_dwordx4 v[188:191], v21, s[40:43], s50 offen offset:1024
buffer_load_dwordx4 v[192:195], v21, s[40:43], s50 offen offset:2048
buffer_load_dwordx4 v[196:199], v21, s[40:43], s50 offen offset:3072
s_add_u32 s50, s50, 4096
s_waitcnt vmcnt(28)
buffer_load_dwordx4 v[200:203], v21, s[40:43], s50 offen offset:0
buffer_load_dwordx4 v[204:207], v21, s[40:43], s50 offen offset:1024
buffer_load_dwordx4 v[208:211], v21, s[40:43], s50 offen offset:2048
buffer_load_dwordx4 v[212:215], v21, s[40:43], s50 offen offset:3072
s_add_u32 s50, s50, 4096
buffer_load_dwordx4 v[216:219], v21, s[40:43], s50 offen offset:0
buffer_load_dwordx4 v[220:223], v21, s[40:43], s50 offen offset:1024
buffer_load_dwordx4 v[224:227], v21, s[40:43], s50 offen offset:2048
buffer_load_dwordx4 v[228:231], v21, s[40:43], s50 offen offset:3072
s_add_u32 s50, s50, 4096
buffer_load_dwordx4 v[232:235], v21, s[40:43], s50 offen offset:0
buffer_load_dwordx4 v[236:239], v21, s[40:43], s50 offen offset:1024
buffer_load_dwordx4 v[240:243], v21, s[40:43], s50 offen offset:2048
buffer_load_dwordx4 v[244:247], v21, s[40:43], s50 offen offset:3072
s_add_u32 s50, s50, 4096
buffer_load_dwordx4 v[248:251], v21, s[40:43], s50 offen offset:0
buffer_load_dwordx4 v[252:255], v21, s[40:43], s50 offen offset:1024
s_waitcnt vmcnt(28)
s_waitcnt vmcnt(0)
s_mov_b32 s65, 0
s_mov_b32 s54, s63
s_add_u32 s81, s63, s84
s_mov_b32 s57, 40000
s_mov_b32 s60, 0
s_mov_b32 s48, s47
s_mov_b32 s49, s61
s_mov_b32 s66, s62
s_mov_b32 s67, s46
s_mul_i32 s82, s44, 7168
s_mov_b32 s83, s82
v_mbcnt_lo_u32_b32 v24, -1, 0
v_min_u32 v24, 5, v24
v_lshlrev_b32 v24, 7, v24
s_add_u32 s50, s66, 1038336
buffer_load_dword v25, v24, s[40:43], s50 offen
s_waitcnt vmcnt(0)
v_readlane_b32 s52, v25, 0
v_readlane_b32 s53, v25, 1
s_cmp_lt_u32 s53, s52
s_cselect_b32 s58, 8192, 0
s_add_u32 s48, s48, s58
v_readlane_b32 s52, v25, 2
v_readlane_b32 s53, v25, 3
s_cmp_lt_u32 s53, s52
s_cselect_b32 s58, 8192, 0
s_add_u32 s49, s49, s58
v_readlane_b32 s52, v25, 4
v_readlane_b32 s53, v25, 5
s_cmp_lt_u32 s53, s52
s_cselect_b32 s58, 8192, 0
s_add_u32 s66, s66, s58
s_add_u32 m0, s83, 0
s_nop 0
buffer_load_dwordx4 v13, s[40:43], s67 offen lds
s_add_u32 m0, s83, 1024
s_nop 0
buffer_load_dwordx4 v14, s[40:43], s67 offen lds
s_add_u32 m0, s83, 2048
s_nop 0
buffer_load_dwordx4 v15, s[40:43], s67 offen lds
s_add_u32 m0, s83, 3072
s_nop 0
buffer_load_dwordx4 v16, s[40:43], s67 offen lds
s_add_u32 m0, s83, 4096
s_nop 0
buffer_load_dwordx4 v17, s[40:43], s67 offen lds
s_add_u32 m0, s83, 5120
s_nop 0
buffer_load_dwordx4 v18, s[40:43], s67 offen lds
s_add_u32 m0, s83, 6144
s_nop 0
buffer_load_dwordx4 v19, s[40:43], s67 offen lds
s_lshl_b32 s58, s44, 2
s_add_u32 s58, s58, 75264
v_mov_b32 v109, s58
v_mov_b32 v108, 75264
s_mov_b32 s56, 0
s_memrealtime s[52:53]
s_waitcnt lgkmcnt(0)
s_mov_b32 s50, s52
s_mov_b32 s99, s52
7:
s_memrealtime s[52:53]
s_memtime s[58:59]
s_waitcnt lgkmcnt(0)
s_cmp_eq_u32 s50, s52
s_cbranch_scc1 7b
s_mov_b32 s50, s52
s_sub_u32 s53, s52, s99
s_mul_i32 s53, s53, 24
s_sub_u32 s53, s53, s58
s_cmp_eq_u32 s56, 0
s_cselect_b32 s95, s53, s95
s_sub_u32 s59, s53, s95
s_cmp_gt_i32 s59, 0
s_cselect_b32 s95, s53, s95
s_add_u32 s56, s56, 1
s_cmp_lt_u32 s56, 8
s_cbranch_scc1 7b
s_mul_i32 s99, s99, 24
s_add_u32 s95, s95, s99
v_mov_b64 v[52:53], 0
v_mov_b64 v[54:55], 0
v_mov_b64 v[56:57], 0
v_mov_b64 v[58:59], 0
v_mov_b64 v[60:61], 0
v_mov_b64 v[62:63], 0
v_mov_b64 v[64:65], 0
v_mov_b64 v[66:67], 0
v_mov_b64 v[68:69], 0
v_mov_b64 v[70:71], 0
v_mov_b64 v[72:73], 0
v_mov_b64 v[74:75], 0
v_mov_b64 v[76:77], 0
v_mov_b64 v[78:79], 0
v_mov_b64 v[80:81], 0
v_mov_b64 v[82:83], 0
s_branch 6f
1:
s_cmp_eq_u32 s44, 3
s_cbranch_scc1 9f
s_sleep 8
9:
v_mbcnt_lo_u32_b32 v24, -1, 0
v_and_b32 v24, 15, v24
v_lshlrev_b32 v24, 7, v24
s_add_u32 s99, s66, -12288
buffer_load_dword v25, v24, s[40:43], s99 offen sc1
s_add_u32 m0, s83, 0
s_nop 0
buffer_load_dwordx4 v13, s[40:43], s67 offen lds
s_add_u32 m0, s83, 1024
s_nop 0
buffer_load_dwordx4 v14, s[40:43], s67 offen lds
s_add_u32 m0, s83, 2048
s_nop 0
buffer_load_dwordx4 v15, s[40:43], s67 offen lds
s_add_u32 m0, s83, 3072
s_nop 0
buffer_load_dwordx4 v16, s[40:43], s67 offen lds
s_add_u32 m0, s83, 4096
s_nop 0
buffer_load_dwordx4 v17, s[40:43], s67 offen lds
s_add_u32 m0, s83, 5120
s_nop 0
buffer_load_dwordx4 v18, s[40:43], s67 offen lds
s_add_u32 m0, s83, 6144
s_nop 0
buffer_load_dwordx4 v19, s[40:43], s67 offen lds
s_mov_b32 s56, 0
s_memtime s[52:53]
s_waitcnt vmcnt(7) lgkmcnt(0)
s_branch 93f
90:
buffer_load_dword v25, v24, s[40:43], s99 offen sc1
s_memtime s[52:53]
s_waitcnt vmcnt(0) lgkmcnt(0)
93:
v_cmp_eq_u32 vcc, -1, v25
s_cbranch_vccz 91f
s_add_u32 s56, s56, 1
s_cmp_lt_u32 s56, s57
s_cbranch_scc0 92f
s_sleep 3
s_branch 90b
92:
s_mov_b32 s57, 1
s_mov_b32 s60, 1
91:
s_add_u32 s58, s52, s95
v_subrev_u32 v25, s58, v25
s_nop 1
v_max_i32_dpp v25, v25, v25 quad_perm:[1,0,3,2] row_mask:0xf bank_mask:0xf
s_nop 1
v_max_i32_dpp v25, v25, v25 quad_perm:[2,3,0,1] row_mask:0xf bank_mask:0xf
s_nop 1
v_max_i32_dpp v25, v25, v25 row_half_mirror row_mask:0xf bank_mask:0xf
s_nop 1
v_max_i32_dpp v25, v25, v25 row_mirror row_mask:0xf bank_mask:0xf
s_nop 1
v_readfirstlane_b32 s55, v25
s_add_u32 s55, s55, s58
v_mov_b64 v[52:53], 0
v_mov_b64 v[54:55], 0
v_mov_b64 v[56:57], 0
v_mov_b64 v[58:59], 0
v_mov_b64 v[60:61], 0
v_mov_b64 v[62:63], 0
v_mov_b64 v[64:65], 0
v_mov_b64 v[66:67], 0
v_mov_b64 v[68:69], 0
v_mov_b64 v[70:71], 0
v_mov_b64 v[72:73], 0
v_mov_b64 v[74:75], 0
v_mov_b64 v[76:77], 0
v_mov_b64 v[78:79], 0
v_mov_b64 v[80:81], 0
v_mov_b64 v[82:83], 0
s_add_u32 s58, s55, s54
s_sub_u32 s58, s58, s95
s_mov_b32 s56, 0
2:
s_memtime s[52:53]
s_add_u32 s56, s56, 1
s_waitcnt lgkmcnt(0)
s_sub_u32 s53, s52, s58
s_cmp_gt_u32 s56, 160
s_cselect_b32 s53, 0, s53
s_cmp_lt_i32 s53, 0
s_cbranch_scc1 2b
6:
s_mov_b32 s56, 0
10:
buffer_load_dwordx4 v[24:27], v3, s[40:43], s48 offen sc1
buffer_load_dwordx4 v[28:31], v3, s[40:43], s48 offen offset:1024 sc1
s_waitcnt vmcnt(0)
s_memtime s[52:53]
ds_write_b128 v4, v[24:27]
ds_write_b128 v5, v[28:31]
v_max3_u32 v21, v24, v25, v26
v_max3_u32 v23, v27, v28, v29
v_max3_u32 v21, v21, v30, v31
v_max_u32 v21, v21, v23
v_cmp_eq_u32 vcc, -1, v21
s_cbranch_vccz 12f
s_add_u32 s56, s56, 1
s_cmp_lt_u32 s56, s57
s_cbranch_scc0 11f
s_sleep 1
s_waitcnt lgkmcnt(0)
s_branch 10b
11:
s_mov_b32 s57, 1
s_mov_b32 s60, 1
12:
s_waitcnt lgkmcnt(0)
s_add_u32 s52, s52, s95
v_mov_b32 v21, s52
ds_write_b32 v109, v21
s_waitcnt lgkmcnt(0)
s_barrier
s_cmp_lg_u32 s44, 3
s_cbranch_scc1 8f
ds_read_b128 v[112:115], v108
8:
ds_read_b64_tr_b16 v[24:25], v2 offset:0
ds_read_b64_tr_b16 v[26:27], v2 offset:256
ds_read_b64_tr_b16 v[28:29], v2 offset:1024
ds_read_b64_tr_b16 v[30:31], v2 offset:1280
ds_read_b64_tr_b16 v[32:33], v2 offset:2048
ds_read_b64_tr_b16 v[34:35], v2 offset:2304
ds_read_b64_tr_b16 v[36:37], v2 offset:3072
ds_read_b64_tr_b16 v[38:39], v2 offset:3328
ds_read_b64_tr_b16 v[40:41], v2 offset:4096
ds_read_b64_tr_b16 v[42:43], v2 offset:4352
ds_read_b64_tr_b16 v[44:45], v2 offset:5120
ds_read_b64_tr_b16 v[46:47], v2 offset:5376
ds_read_b64_tr_b16 v[48:49], v2 offset:6144
ds_read_b64_tr_b16 v[50:51], v2 offset:6400
s_add_u32 s50, s66, s80
s_cmp_lt_u32 s65, 3
s_cbranch_scc1 4f
s_cmp_eq_u32 s56, 0
s_cbranch_scc0 3f
s_sub_u32 s54, s54, s85
s_branch 4f
3:
s_add_u32 s54, s54, s94
s_min_i32 s54, s54, s81
4:
s_cmp_ge_u32 s44, 2
s_cbranch_scc1 32f
s_waitcnt lgkmcnt(12)
v_smfmac_f32_16x16x64_bf16 v[52:55], v[24:27], a[0:7], v1
s_waitcnt lgkmcnt(10)
v_smfmac_f32_16x16x64_bf16 v[52:55], v[28:31], a[8:15], v1
s_waitcnt lgkmcnt(8)
v_smfmac_f32_16x16x64_bf16 v[52:55], v[32:35], a[16:23], v1
s_waitcnt lgkmcnt(6)
v_smfmac_f32_16x16x64_bf16 v[52:55], v[36:39], a[24:31], v1
s_waitcnt lgkmcnt(4)
v_smfmac_f32_16x16x64_bf16 v[52:55], v[40:43], a[32:39], v1
s_waitcnt lgkmcnt(2)
v_smfmac_f32_16x16x64_bf16 v[52:55], v[44:47], a[40:47], v1
s_waitcnt lgkmcnt(0)
v_smfmac_f32_16x16x64_bf16 v[52:55], v[48:51], a[48:55], v1
v_smfmac_f32_16x16x64_bf16 v[56:59], v[24:27], a[56:63], v1
v_smfmac_f32_16x16x64_bf16 v[56:59], v[28:31], a[64:71], v1
v_add_f32 v21, v52, v53
v_add_f32 v23, v54, v55
v_smfmac_f32_16x16x64_bf16 v[56:59], v[32:35], a[72:79], v1
v_cvt_pk_bf16_f32 v21, v21, v23
v_smfmac_f32_16x16x64_bf16 v[56:59], v[36:39], a[80:87], v1
ds_write_b16 v6, v21 offset:0
v_smfmac_f32_16x16x64_bf16 v[56:59], v[40:43], a[88:95], v1
ds_write_b16_d16_hi v6, v21 offset:4
v_smfmac_f32_16x16x64_bf16 v[56:59], v[44:47], a[96:103], v1
ds_read_b128 v[92:95], v7 offset:0
v_smfmac_f32_16x16x64_bf16 v[56:59], v[48:51], a[104:111], v1
ds_read_b128 v[96:99], v0 offset:0
s_waitcnt lgkmcnt(2)
s_barrier
v_smfmac_f32_16x16x64_bf16 v[60:63], v[24:27], a[112:119], v1
ds_read_b64_tr_b16 v[84:85], v2 offset:7168
ds_read_b64_tr_b16 v[86:87], v2 offset:7424
v_smfmac_f32_16x16x64_bf16 v[60:63], v[28:31], a[120:127], v1
v_add_f32 v21, v56, v57
v_add_f32 v23, v58, v59
v_smfmac_f32_16x16x64_bf16 v[60:63], v[32:35], a[128:135], v1
v_cvt_pk_bf16_f32 v21, v21, v23
v_smfmac_f32_16x16x64_bf16 v[60:63], v[36:39], a[136:143], v1
ds_write_b16 v6, v21 offset:1024
v_smfmac_f32_16x16x64_bf16 v[60:63], v[40:43], a[144:151], v1
ds_write_b16_d16_hi v6, v21 offset:1028
v_smfmac_f32_16x16x64_bf16 v[60:63], v[44:47], a[152:159], v1
ds_read_b128 v[100:103], v7 offset:128
v_smfmac_f32_16x16x64_bf16 v[60:63], v[48:51], a[160:167], v1
ds_read_b128 v[104:107], v0 offset:128
s_waitcnt lgkmcnt(4)
v_smfmac_f32_16x16x64_bf16 v[80:83], v[84:87], v[92:99], v1
s_waitcnt lgkmcnt(2)
s_barrier
v_smfmac_f32_16x16x64_bf16 v[64:67], v[24:27], a[168:175], v1
ds_read_b64_tr_b16 v[88:89], v2 offset:8192
ds_read_b64_tr_b16 v[90:91], v2 offset:8448
v_smfmac_f32_16x16x64_bf16 v[64:67], v[28:31], a[176:183], v1
v_add_f32 v21, v60, v61
v_add_f32 v23, v62, v63
v_smfmac_f32_16x16x64_bf16 v[64:67], v[32:35], a[184:191], v1
v_cvt_pk_bf16_f32 v21, v21, v23
v_smfmac_f32_16x16x64_bf16 v[64:67], v[36:39], a[192:199], v1
ds_write_b16 v6, v21 offset:2048
v_smfmac_f32_16x16x64_bf16 v[64:67], v[40:43], a[200:207], v1
ds_write_b16_d16_hi v6, v21 offset:2052
v_smfmac_f32_16x16x64_bf16 v[64:67], v[44:47], a[208:215], v1
ds_read_b128 v[92:95], v7 offset:256
v_smfmac_f32_16x16x64_bf16 v[64:67], v[48:51], a[216:223], v1
ds_read_b128 v[96:99], v0 offset:256
s_waitcnt lgkmcnt(4)
v_smfmac_f32_16x16x64_bf16 v[80:83], v[88:91], v[100:107], v1
s_waitcnt lgkmcnt(2)
s_barrier
v_smfmac_f32_16x16x64_bf16 v[68:71], v[24:27], a[224:231], v1
ds_read_b64_tr_b16 v[84:85], v2 offset:9216
ds_read_b64_tr_b16 v[86:87], v2 offset:9472
v_smfmac_f32_16x16x64_bf16 v[68:71], v[28:31], a[232:239], v1
v_add_f32 v21, v64, v65
v_add_f32 v23, v66, v67
v_smfmac_f32_16x16x64_bf16 v[68:71], v[32:35], a[240:247], v1
v_cvt_pk_bf16_f32 v21, v21, v23
v_smfmac_f32_16x16x64_bf16 v[68:71], v[36:39], a[248:255], v1
ds_write_b16 v6, v21 offset:3072
v_smfmac_f32_16x16x64_bf16 v[68:71], v[40:43], v[120:127], v1
ds_write_b16_d16_hi v6, v21 offset:3076
v_smfmac_f32_16x16x64_bf16 v[68:71], v[44:47], v[128:135], v1
ds_read_b128 v[100:103], v7 offset:384
v_smfmac_f32_16x16x64_bf16 v[68:71], v[48:51], v[136:143], v1
ds_read_b128 v[104:107], v0 offset:384
s_waitcnt lgkmcnt(4)
v_smfmac_f32_16x16x64_bf16 v[80:83], v[84:87], v[92:99], v1
s_waitcnt lgkmcnt(2)
s_barrier
v_smfmac_f32_16x16x64_bf16 v[72:75], v[24:27], v[144:151], v1
ds_read_b64_tr_b16 v[88:89], v2 offset:10240
ds_read_b64_tr_b16 v[90:91], v2 offset:10496
v_smfmac_f32_16x16x64_bf16 v[72:75], v[28:31], v[152:159], v1
v_add_f32 v21, v68, v69
v_add_f32 v23, v70, v71
v_smfmac_f32_16x16x64_bf16 v[72:75], v[32:35], v[160:167], v1
v_cvt_pk_bf16_f32 v21, v21, v23
v_smfmac_f32_16x16x64_bf16 v[72:75], v[36:39], v[168:175], v1
ds_write_b16 v6, v21 offset:4096
v_smfmac_f32_16x16x64_bf16 v[72:75], v[40:43], v[176:183], v1
ds_write_b16_d16_hi v6, v21 offset:4100
v_smfmac_f32_16x16x64_bf16 v[72:75], v[44:47], v[184:191], v1
ds_read_b128 v[92:95], v7 offset:512
v_smfmac_f32_16x16x64_bf16 v[72:75], v[48:51], v[192:199], v1
ds_read_b128 v[96:99], v0 offset:512
s_waitcnt lgkmcnt(4)
v_smfmac_f32_16x16x64_bf16 v[80:83], v[88:91], v[100:107], v1
s_waitcnt lgkmcnt(2)
s_barrier
v_smfmac_f32_16x16x64_bf16 v[76:79], v[24:27], v[200:207], v1
ds_read_b64_tr_b16 v[84:85], v2 offset:11264
ds_read_b64_tr_b16 v[86:87], v2 offset:11520
v_smfmac_f32_16x16x64_bf16 v[76:79], v[28:31], v[208:215], v1
v_add_f32 v21, v72, v73
v_add_f32 v23, v74, v75
v_smfmac_f32_16x16x64_bf16 v[76:79], v[32:35], v[216:223], v1
v_cvt_pk_bf16_f32 v21, v21, v23
v_smfmac_f32_16x16x64_bf16 v[76:79], v[36:39], v[224:231], v1
ds_write_b16 v6, v21 offset:5120
v_smfmac_f32_16x16x64_bf16 v[76:79], v[40:43], v[232:239], v1
ds_write_b16_d16_hi v6, v21 offset:5124
v_smfmac_f32_16x16x64_bf16 v[76:79], v[44:47], v[240:247], v1
ds_read_b128 v[100:103], v7 offset:640
v_smfmac_f32_16x16x64_bf16 v[76:79], v[48:51], v[248:255], v1
ds_read_b128 v[104:107], v0 offset:640
s_waitcnt lgkmcnt(4)
v_smfmac_f32_16x16x64_bf16 v[80:83], v[84:87], v[92:99], v1
ds_read_b128 v[92:95], v7 offset:768
ds_read_b128 v[96:99], v0 offset:768
s_waitcnt lgkmcnt(4)
s_barrier
ds_read_b64_tr_b16 v[88:89], v2 offset:12288
ds_read_b64_tr_b16 v[90:91], v2 offset:12544
s_nop 3
v_add_f32 v21, v76, v77
v_add_f32 v23, v78, v79
v_cvt_pk_bf16_f32 v21, v21, v23
ds_write_b16 v6, v21 offset:6144
ds_write_b16_d16_hi v6, v21 offset:6148
s_waitcnt lgkmcnt(2)
v_smfmac_f32_16x16x64_bf16 v[80:83], v[88:91], v[100:107], v1
s_waitcnt lgkmcnt(0)
s_barrier
ds_read_b64_tr_b16 v[84:85], v2 offset:13312
ds_read_b64_tr_b16 v[86:87], v2 offset:13568
s_waitcnt lgkmcnt(0)
v_smfmac_f32_16x16x64_bf16 v[80:83], v[84:87], v[92:99], v1
s_nop 7
v_add_f32 v80, v80, v81
v_add_f32 v81, v82, v83
ds_write_b64 v8, v[80:81]
s_waitcnt lgkmcnt(0)
s_barrier
s_branch 40f
32:
s_cmp_eq_u32 s44, 3
s_cbranch_scc1 33f
s_waitcnt lgkmcnt(12)
v_smfmac_f32_16x16x64_bf16 v[52:55], v[24:27], a[0:7], v1
s_waitcnt lgkmcnt(10)
v_smfmac_f32_16x16x64_bf16 v[52:55], v[28:31], a[8:15], v1
s_waitcnt lgkmcnt(8)
v_smfmac_f32_16x16x64_bf16 v[52:55], v[32:35], a[16:23], v1
s_waitcnt lgkmcnt(6)
v_smfmac_f32_16x16x64_bf16 v[52:55], v[36:39], a[24:31], v1
s_waitcnt lgkmcnt(4)
v_smfmac_f32_16x16x64_bf16 v[52:55], v[40:43], a[32:39], v1
s_waitcnt lgkmcnt(2)
v_smfmac_f32_16x16x64_bf16 v[52:55], v[44:47], a[40:47], v1
s_waitcnt lgkmcnt(0)
v_smfmac_f32_16x16x64_bf16 v[52:55], v[48:51], a[48:55], v1
v_smfmac_f32_16x16x64_bf16 v[56:59], v[24:27], a[56:63], v1
ds_read_b64_tr_b16 v[92:93], v7 offset:0
v_smfmac_f32_16x16x64_bf16 v[56:59], v[28:31], a[64:71], v1
v_add_f32 v21, v52, v53
v_add_f32 v23, v54, v55
v_smfmac_f32_16x16x64_bf16 v[56:59], v[32:35], a[72:79], v1
v_cvt_pk_bf16_f32 v21, v21, v23
v_smfmac_f32_16x16x64_bf16 v[56:59], v[36:39], a[80:87], v1
ds_write_b16 v6, v21 offset:0
v_smfmac_f32_16x16x64_bf16 v[56:59], v[40:43], a[88:95], v1
ds_write_b16_d16_hi v6, v21 offset:4
v_smfmac_f32_16x16x64_bf16 v[56:59], v[44:47], a[96:103], v1
ds_read_b64_tr_b16 v[94:95], v7 offset:64
v_smfmac_f32_16x16x64_bf16 v[56:59], v[48:51], a[104:111], v1
ds_read_b64_tr_b16 v[96:97], v7 offset:512
ds_read_b64_tr_b16 v[98:99], v7 offset:576
s_waitcnt lgkmcnt(3)
s_barrier
v_smfmac_f32_16x16x64_bf16 v[60:63], v[24:27], a[112:119], v1
ds_read_b64_tr_b16 v[84:85], v2 offset:7168
ds_read_b64_tr_b16 v[86:87], v2 offset:7424
v_smfmac_f32_16x16x64_bf16 v[60:63], v[28:31], a[120:127], v1
v_add_f32 v21, v56, v57
v_add_f32 v23, v58, v59
v_smfmac_f32_16x16x64_bf16 v[60:63], v[32:35], a[128:135], v1
v_cvt_pk_bf16_f32 v21, v21, v23
v_smfmac_f32_16x16x64_bf16 v[60:63], v[36:39], a[136:143], v1
ds_write_b16 v6, v21 offset:1024
v_smfmac_f32_16x16x64_bf16 v[60:63], v[40:43], a[144:151], v1
ds_write_b16_d16_hi v6, v21 offset:1028
v_smfmac_f32_16x16x64_bf16 v[60:63], v[44:47], a[152:159], v1
v_smfmac_f32_16x16x64_bf16 v[60:63], v[48:51], a[160:167], v1
s_waitcnt lgkmcnt(2)
v_smfmac_f32_16x16x64_bf16 v[80:83], v[84:87], v[92:99], v1
s_waitcnt lgkmcnt(0)
s_barrier
v_smfmac_f32_16x16x64_bf16 v[64:67], v[24:27], a[168:175], v1
ds_read_b64_tr_b16 v[92:93], v7 offset:2048
v_smfmac_f32_16x16x64_bf16 v[64:67], v[28:31], a[176:183], v1
v_add_f32 v21, v60, v61
v_add_f32 v23, v62, v63
v_smfmac_f32_16x16x64_bf16 v[64:67], v[32:35], a[184:191], v1
v_cvt_pk_bf16_f32 v21, v21, v23
v_smfmac_f32_16x16x64_bf16 v[64:67], v[36:39], a[192:199], v1
ds_write_b16 v6, v21 offset:2048
v_smfmac_f32_16x16x64_bf16 v[64:67], v[40:43], a[200:207], v1
ds_write_b16_d16_hi v6, v21 offset:2052
v_smfmac_f32_16x16x64_bf16 v[64:67], v[44:47], a[208:215], v1
ds_read_b64_tr_b16 v[94:95], v7 offset:2112
v_smfmac_f32_16x16x64_bf16 v[64:67], v[48:51], a[216:223], v1
ds_read_b64_tr_b16 v[96:97], v7 offset:2560
ds_read_b64_tr_b16 v[98:99], v7 offset:2624
s_waitcnt lgkmcnt(3)
s_barrier
v_smfmac_f32_16x16x64_bf16 v[68:71], v[24:27], a[224:231], v1
ds_read_b64_tr_b16 v[84:85], v2 offset:9216
ds_read_b64_tr_b16 v[86:87], v2 offset:9472
v_smfmac_f32_16x16x64_bf16 v[68:71], v[28:31], a[232:239], v1
v_add_f32 v21, v64, v65
v_add_f32 v23, v66, v67
v_smfmac_f32_16x16x64_bf16 v[68:71], v[32:35], a[240:247], v1
v_cvt_pk_bf16_f32 v21, v21, v23
v_smfmac_f32_16x16x64_bf16 v[68:71], v[36:39], a[248:255], v1
ds_write_b16 v6, v21 offset:3072
v_smfmac_f32_16x16x64_bf16 v[68:71], v[40:43], v[120:127], v1
ds_write_b16_d16_hi v6, v21 offset:3076
v_smfmac_f32_16x16x64_bf16 v[68:71], v[44:47], v[128:135], v1
v_smfmac_f32_16x16x64_bf16 v[68:71], v[48:51], v[136:143], v1
s_waitcnt lgkmcnt(2)
v_smfmac_f32_16x16x64_bf16 v[80:83], v[84:87], v[92:99], v1
s_waitcnt lgkmcnt(0)
s_barrier
v_smfmac_f32_16x16x64_bf16 v[72:75], v[24:27], v[144:151], v1
ds_read_b64_tr_b16 v[92:93], v7 offset:4096
v_smfmac_f32_16x16x64_bf16 v[72:75], v[28:31], v[152:159], v1
v_add_f32 v21, v68, v69
v_add_f32 v23, v70, v71
v_smfmac_f32_16x16x64_bf16 v[72:75], v[32:35], v[160:167], v1
v_cvt_pk_bf16_f32 v21, v21, v23
v_smfmac_f32_16x16x64_bf16 v[72:75], v[36:39], v[168:175], v1
ds_write_b16 v6, v21 offset:4096
v_smfmac_f32_16x16x64_bf16 v[72:75], v[40:43], v[176:183], v1
ds_write_b16_d16_hi v6, v21 offset:4100
v_smfmac_f32_16x16x64_bf16 v[72:75], v[44:47], v[184:191], v1
ds_read_b64_tr_b16 v[94:95], v7 offset:4160
v_smfmac_f32_16x16x64_bf16 v[72:75], v[48:51], v[192:199], v1
ds_read_b64_tr_b16 v[96:97], v7 offset:4608
ds_read_b64_tr_b16 v[98:99], v7 offset:4672
s_waitcnt lgkmcnt(3)
s_barrier
v_smfmac_f32_16x16x64_bf16 v[76:79], v[24:27], v[200:207], v1
ds_read_b64_tr_b16 v[84:85], v2 offset:11264
ds_read_b64_tr_b16 v[86:87], v2 offset:11520
v_smfmac_f32_16x16x64_bf16 v[76:79], v[28:31], v[208:215], v1
v_add_f32 v21, v72, v73
v_add_f32 v23, v74, v75
v_smfmac_f32_16x16x64_bf16 v[76:79], v[32:35], v[216:223], v1
v_cvt_pk_bf16_f32 v21, v21, v23
v_smfmac_f32_16x16x64_bf16 v[76:79], v[36:39], v[224:231], v1
ds_write_b16 v6, v21 offset:5120
v_smfmac_f32_16x16x64_bf16 v[76:79], v[40:43], v[232:239], v1
ds_write_b16_d16_hi v6, v21 offset:5124
v_smfmac_f32_16x16x64_bf16 v[76:79], v[44:47], v[240:247], v1
v_smfmac_f32_16x16x64_bf16 v[76:79], v[48:51], v[248:255], v1
s_waitcnt lgkmcnt(2)
v_smfmac_f32_16x16x64_bf16 v[80:83], v[84:87], v[92:99], v1
ds_read_b64_tr_b16 v[92:93], v7 offset:6144
ds_read_b64_tr_b16 v[94:95], v7 offset:6208
ds_read_b64_tr_b16 v[96:97], v7 offset:6656
ds_read_b64_tr_b16 v[98:99], v7 offset:6720
s_waitcnt lgkmcnt(4)
s_barrier
s_nop 3
v_add_f32 v21, v76, v77
v_add_f32 v23, v78, v79
v_cvt_pk_bf16_f32 v21, v21, v23
ds_write_b16 v6, v21 offset:6144
ds_write_b16_d16_hi v6, v21 offset:6148
s_waitcnt lgkmcnt(0)
s_barrier
ds_read_b64_tr_b16 v[84:85], v2 offset:13312
ds_read_b64_tr_b16 v[86:87], v2 offset:13568
s_waitcnt lgkmcnt(0)
v_smfmac_f32_16x16x64_bf16 v[80:83], v[84:87], v[92:99], v1
s_nop 7
v_add_f32_dpp v80, v81, v80 quad_perm:[1,0,3,2] row_mask:0xf bank_mask:0xf
v_add_f32_dpp v81, v83, v82 quad_perm:[1,0,3,2] row_mask:0xf bank_mask:0xf
ds_write_b64 v8, v[80:81]
s_waitcnt lgkmcnt(0)
s_barrier
s_branch 40f
33:
s_waitcnt lgkmcnt(12)
v_smfmac_f32_16x16x64_bf16 v[52:55], v[24:27], a[0:7], v1
s_waitcnt lgkmcnt(10)
v_smfmac_f32_16x16x64_bf16 v[52:55], v[28:31], a[8:15], v1
v_max3_u32 v112, v112, v113, v114
s_waitcnt lgkmcnt(8)
v_smfmac_f32_16x16x64_bf16 v[52:55], v[32:35], a[16:23], v1
v_max_u32 v112, v112, v115
s_waitcnt lgkmcnt(6)
v_smfmac_f32_16x16x64_bf16 v[52:55], v[36:39], a[24:31], v1
v_min_u32 v112, -2, v112
s_waitcnt lgkmcnt(4)
v_smfmac_f32_16x16x64_bf16 v[52:55], v[40:43], a[32:39], v1
buffer_store_dword v112, v22, s[40:43], s50 offen sc1
s_waitcnt lgkmcnt(2)
v_smfmac_f32_16x16x64_bf16 v[52:55], v[44:47], a[40:47], v1
s_waitcnt lgkmcnt(0)
v_smfmac_f32_16x16x64_bf16 v[52:55], v[48:51], a[48:55], v1
v_smfmac_f32_16x16x64_bf16 v[56:59], v[24:27], a[56:63], v1
v_smfmac_f32_16x16x64_bf16 v[56:59], v[28:31], a[64:71], v1
v_add_f32 v21, v52, v53
v_add_f32 v23, v54, v55
v_smfmac_f32_16x16x64_bf16 v[56:59], v[32:35], a[72:79], v1
v_cvt_pk_bf16_f32 v21, v21, v23
v_smfmac_f32_16x16x64_bf16 v[56:59], v[36:39], a[80:87], v1
ds_write_b16 v6, v21 offset:0
v_smfmac_f32_16x16x64_bf16 v[56:59], v[40:43], a[88:95], v1
ds_write_b16_d16_hi v6, v21 offset:4
v_smfmac_f32_16x16x64_bf16 v[56:59], v[44:47], a[96:103], v1
v_smfmac_f32_16x16x64_bf16 v[56:59], v[48:51], a[104:111], v1
s_waitcnt lgkmcnt(0)
s_barrier
v_smfmac_f32_16x16x64_bf16 v[60:63], v[24:27], a[112:119], v1
ds_read_b64_tr_b16 v[100:101], v7 offset:1024
v_smfmac_f32_16x16x64_bf16 v[60:63], v[28:31], a[120:127], v1
v_add_f32 v21, v56, v57
v_add_f32 v23, v58, v59
v_smfmac_f32_16x16x64_bf16 v[60:63], v[32:35], a[128:135], v1
v_cvt_pk_bf16_f32 v21, v21, v23
v_smfmac_f32_16x16x64_bf16 v[60:63], v[36:39], a[136:143], v1
ds_write_b16 v6, v21 offset:1024
v_smfmac_f32_16x16x64_bf16 v[60:63], v[40:43], a[144:151], v1
ds_write_b16_d16_hi v6, v21 offset:1028
v_smfmac_f32_16x16x64_bf16 v[60:63], v[44:47], a[152:159], v1
ds_read_b64_tr_b16 v[102:103], v7 offset:1088
v_smfmac_f32_16x16x64_bf16 v[60:63], v[48:51], a[160:167], v1
ds_read_b64_tr_b16 v[104:105], v7 offset:1536
ds_read_b64_tr_b16 v[106:107], v7 offset:1600
s_waitcnt lgkmcnt(3)
s_barrier
v_smfmac_f32_16x16x64_bf16 v[64:67], v[24:27], a[168:175], v1
ds_read_b64_tr_b16 v[88:89], v2 offset:8192
ds_read_b64_tr_b16 v[90:91], v2 offset:8448
v_smfmac_f32_16x16x64_bf16 v[64:67], v[28:31], a[176:183], v1
v_add_f32 v21, v60, v61
v_add_f32 v23, v62, v63
v_smfmac_f32_16x16x64_bf16 v[64:67], v[32:35], a[184:191], v1
v_cvt_pk_bf16_f32 v21, v21, v23
v_smfmac_f32_16x16x64_bf16 v[64:67], v[36:39], a[192:199], v1
ds_write_b16 v6, v21 offset:2048
v_smfmac_f32_16x16x64_bf16 v[64:67], v[40:43], a[200:207], v1
ds_write_b16_d16_hi v6, v21 offset:2052
v_smfmac_f32_16x16x64_bf16 v[64:67], v[44:47], a[208:215], v1
v_smfmac_f32_16x16x64_bf16 v[64:67], v[48:51], a[216:223], v1
s_waitcnt lgkmcnt(2)
v_smfmac_f32_16x16x64_bf16 v[80:83], v[88:91], v[100:107], v1
s_waitcnt lgkmcnt(0)
s_barrier
v_smfmac_f32_16x16x64_bf16 v[68:71], v[24:27], a[224:231], v1
ds_read_b64_tr_b16 v[100:101], v7 offset:3072
v_smfmac_f32_16x16x64_bf16 v[68:71], v[28:31], a[232:239], v1
v_add_f32 v21, v64, v65
v_add_f32 v23, v66, v67
v_smfmac_f32_16x16x64_bf16 v[68:71], v[32:35], a[240:247], v1
v_cvt_pk_bf16_f32 v21, v21, v23
v_smfmac_f32_16x16x64_bf16 v[68:71], v[36:39], a[248:255], v1
ds_write_b16 v6, v21 offset:3072
v_smfmac_f32_16x16x64_bf16 v[68:71], v[40:43], v[120:127], v1
ds_write_b16_d16_hi v6, v21 offset:3076
v_smfmac_f32_16x16x64_bf16 v[68:71], v[44:47], v[128:135], v1
ds_read_b64_tr_b16 v[102:103], v7 offset:3136
v_smfmac_f32_16x16x64_bf16 v[68:71], v[48:51], v[136:143], v1
ds_read_b64_tr_b16 v[104:105], v7 offset:3584
ds_read_b64_tr_b16 v[106:107], v7 offset:3648
s_waitcnt lgkmcnt(3)
s_barrier
v_smfmac_f32_16x16x64_bf16 v[72:75], v[24:27], v[144:151], v1
ds_read_b64_tr_b16 v[88:89], v2 offset:10240
ds_read_b64_tr_b16 v[90:91], v2 offset:10496
v_smfmac_f32_16x16x64_bf16 v[72:75], v[28:31], v[152:159], v1
v_add_f32 v21, v68, v69
v_add_f32 v23, v70, v71
v_smfmac_f32_16x16x64_bf16 v[72:75], v[32:35], v[160:167], v1
v_cvt_pk_bf16_f32 v21, v21, v23
v_smfmac_f32_16x16x64_bf16 v[72:75], v[36:39], v[168:175], v1
ds_write_b16 v6, v21 offset:4096
v_smfmac_f32_16x16x64_bf16 v[72:75], v[40:43], v[176:183], v1
ds_write_b16_d16_hi v6, v21 offset:4100
v_smfmac_f32_16x16x64_bf16 v[72:75], v[44:47], v[184:191], v1
v_smfmac_f32_16x16x64_bf16 v[72:75], v[48:51], v[192:199], v1
s_waitcnt lgkmcnt(2)
v_smfmac_f32_16x16x64_bf16 v[80:83], v[88:91], v[100:107], v1
s_waitcnt lgkmcnt(0)
s_barrier
v_smfmac_f32_16x16x64_bf16 v[76:79], v[24:27], v[200:207], v1
ds_read_b64_tr_b16 v[100:101], v7 offset:5120
v_smfmac_f32_16x16x64_bf16 v[76:79], v[28:31], v[208:215], v1
v_add_f32 v21, v72, v73
v_add_f32 v23, v74, v75
v_smfmac_f32_16x16x64_bf16 v[76:79], v[32:35], v[216:223], v1
v_cvt_pk_bf16_f32 v21, v21, v23
v_smfmac_f32_16x16x64_bf16 v[76:79], v[36:39], v[224:231], v1
ds_write_b16 v6, v21 offset:5120
v_smfmac_f32_16x16x64_bf16 v[76:79], v[40:43], v[232:239], v1
ds_write_b16_d16_hi v6, v21 offset:5124
v_smfmac_f32_16x16x64_bf16 v[76:79], v[44:47], v[240:247], v1
ds_read_b64_tr_b16 v[102:103], v7 offset:5184
v_smfmac_f32_16x16x64_bf16 v[76:79], v[48:51], v[248:255], v1
ds_read_b64_tr_b16 v[104:105], v7 offset:5632
ds_read_b64_tr_b16 v[106:107], v7 offset:5696
s_waitcnt lgkmcnt(3)
s_barrier
ds_read_b64_tr_b16 v[88:89], v2 offset:12288
ds_read_b64_tr_b16 v[90:91], v2 offset:12544
s_nop 3
v_add_f32 v21, v76, v77
v_add_f32 v23, v78, v79
v_cvt_pk_bf16_f32 v21, v21, v23
ds_write_b16 v6, v21 offset:6144
ds_write_b16_d16_hi v6, v21 offset:6148
s_waitcnt lgkmcnt(2)
v_smfmac_f32_16x16x64_bf16 v[80:83], v[88:91], v[100:107], v1
s_waitcnt lgkmcnt(0)
s_barrier
s_nop 7
v_add_f32_dpp v80, v81, v80 quad_perm:[1,0,3,2] row_mask:0xf bank_mask:0xf
v_add_f32_dpp v81, v83, v82 quad_perm:[1,0,3,2] row_mask:0xf bank_mask:0xf
ds_write_b64 v8, v[80:81]
s_waitcnt lgkmcnt(0)
s_barrier
ds_read_b32 v92, v9
ds_read_b32 v93, v9 offset:40
ds_read_b32 v94, v9 offset:640
ds_read_b32 v95, v9 offset:680
ds_read_b32 v96, v10
ds_read_b32 v97, v20
s_waitcnt lgkmcnt(0)
v_add_f32 v96, v96, v97
v_max3_f32 v98, v92, v93, v94
v_max3_f32 v98, v98, v95, v96
s_nop 1
v_max_f32_dpp v98, v98, v98 quad_perm:[1,0,3,2] row_mask:0xf bank_mask:0xf
s_nop 1
v_max_f32_dpp v98, v98, v98 quad_perm:[2,3,0,1] row_mask:0xf bank_mask:0xf
s_nop 1
v_max_f32_dpp v98, v98, v98 row_half_mirror row_mask:0xf bank_mask:0xf
v_sub_f32 v92, v92, v98
v_sub_f32 v93, v93, v98
v_sub_f32 v94, v94, v98
v_sub_f32 v95, v95, v98
v_sub_f32 v96, v96, v98
v_exp_f32 v92, v92
v_exp_f32 v93, v93
v_exp_f32 v94, v94
v_exp_f32 v95, v95
v_exp_f32 v96, v96
v_add_f32 v99, v92, v93
v_add_f32 v100, v94, v95
v_add_f32 v99, v99, v96
v_add_f32 v99, v99, v100
s_nop 1
v_add_f32_dpp v99, v99, v99 quad_perm:[1,0,3,2] row_mask:0xf bank_mask:0xf
s_nop 1
v_add_f32_dpp v99, v99, v99 quad_perm:[2,3,0,1] row_mask:0xf bank_mask:0xf
s_nop 1
v_add_f32_dpp v99, v99, v99 row_half_mirror row_mask:0xf bank_mask:0xf
v_rcp_f32 v99, v99
s_nop 0
v_mul_f32 v92, v92, v99
v_mul_f32 v93, v93, v99
v_mul_f32 v94, v94, v99
v_mul_f32 v95, v95, v99
v_mul_f32 v96, v96, v99
v_cvt_pk_bf16_f32 v100, v92, v93
v_cvt_pk_bf16_f32 v101, v94, v95
v_mov_b32_dpp v102, v96 quad_perm:[1,0,3,2] row_mask:0xf bank_mask:0xf
v_min_u32 v100, -2, v100
v_min_u32 v101, -2, v101
v_cvt_pk_bf16_f32 v102, v96, v102
buffer_store_dwordx2 v[100:101], v11, s[40:43], s49 offen sc1
v_min_u32 v102, -2, v102
buffer_store_dword v102, v12, s[40:43], s66 offen sc1
40:
s_add_u32 s65, s65, 1
s_add_u32 s48, s48, 16384
s_add_u32 s49, s49, 16384
s_add_u32 s66, s66, 16384
s_add_u32 s67, s67, 28672
s_cmp_lt_u32 s65, s64
s_cbranch_scc1 1b
s_waitcnt vmcnt(0)
s_cmp_eq_u32 s60, 0
s_cbranch_scc1 99f
v_mov_b32 v24, 0x111
buffer_store_dword v24, v22, s[40:43], s69 offen sc1
s_waitcnt vmcnt(0)
99:
)ASM"
        : "+{v0}"(a0), "+{v7}"(a7), "+{v21}"(a21)
        : "{v1}"(idx), "{v2}"(a2), "{v3}"(a3), "{v4}"(a4), "{v5}"(a5), "{v6}"(a6), "{v8}"(a8), "{v9}"(a9), "{v10}"(a10), "{v11}"(a11), "{v12}"(a12),
          "{v13}"(xa[0]), "{v14}"(xa[1]), "{v15}"(xa[2]), "{v16}"(xa[3]), "{v17}"(xa[4]), "{v18}"(xa[5]), "{v19}"(xa[6]), "{v20}"(a20), "{v22}"(a22),
          "{s40}"((unsigned)wsa), "{s41}"((unsigned)(wsa >> 32) & 0xffffu), "{s42}"(0x7fffffffu), "{s43}"(0x00020000u),
          "{s44}"(w), "{s45}"((unsigned)(OFF_AT + (size_t)h * D * D * 2 + w * 98 * 1024)), "{s46}"((unsigned)OFF_XN), "{s47}"((unsigned)OFF_WM),
          "{s61}"((unsigned)OFF_WPX), "{s62}"((unsigned)OFF_WPM), "{s63}"(p.dS), "{s64}"(NSEG - 1),
          "{s68}"((unsigned)(OFF_DBG + (h * 4 + w) * 64)), "{s69}"((unsigned)OFF_ERR), "{s84}"(p.fast), "{s85}"(p.slow), "{s94}"(p.up), "{s80}"((unsigned)(h * 128 + 4096))
        : "v23", "v24", "v25", "v26", "v27", "v28", "v29", "v30", "v31", "v32", "v33", "v34", "v35", "v36", "v37", "v38", "v39", "v40", "v41", "v42", "v43", "v44", "v45", "v46", "v47", "v48", "v49", "v50", "v51", "v52", "v53", "v54", "v55", "v56", "v57", "v58", "v59", "v60", "v61", "v62", "v63", "v64", "v65", "v66", "v67", "v68", "v69", "v70", "v71", "v72", "v73", "v74", "v75", "v76", "v77", "v78", "v79", "v80", "v81", "v82", "v83", "v84", "v85", "v86", "v87", "v88", "v89", "v90", "v91", "v92", "v93", "v94", "v95", "v96", "v97", "v98", "v99", "v100", "v101", "v102", "v103", "v104", "v105", "v106", "v107", "v108", "v109", "v110", "v111", "v112", "v113", "v114", "v115", "v116", "v117", "v118", "v119", "v120", "v121", "v122", "v123", "v124", "v125", "v126", "v127", "v128", "v129", "v130", "v131", "v132", "v133", "v134", "v135", "v136", "v137", "v138", "v139", "v140", "v141", "v142", "v143", "v144", "v145", "v146", "v147", "v148", "v149", "v150", "v151", "v152", "v153", "v154", "v155", "v156", "v157", "v158", "v159", "v160", "v161", "v162", "v163", "v164", "v165", "v166", "v167", "v168", "v169", "v170", "v171", "v172", "v173", "v174", "v175", "v176", "v177", "v178", "v179", "v180", "v181", "v182", "v183", "v184", "v185", "v186", "v187", "v188", "v189", "v190", "v191", "v192", "v193", "v194", "v195", "v196", "v197", "v198", "v199", "v200", "v201", "v202", "v203", "v204", "v205", "v206", "v207", "v208", "v209", "v210", "v211", "v212", "v213", "v214", "v215", "v216", "v217", "v218", "v219", "v220", "v221", "v222", "v223", "v224", "v225", "v226", "v227", "v228", "v229", "v230", "v231", "v232", "v233", "v234", "v235", "v236", "v237", "v238", "v239", "v240", "v241", "v242", "v243", "v244", "v245", "v246", "v247", "v248", "v249", "v250", "v251", "v252", "v253", "v254", "v255", "a0", "a1", "a2", "a3", "a4", "a5", "a6", "a7", "a8", "a9", "a10", "a11", "a12", "a13", "a14", "a15", "a16", "a17", "a18", "a19", "a20", "a21", "a22", "a23", "a24", "a25", "a26", "a27", "a28", "a29", "a30", "a31", "a32", "a33", "a34", "a35", "a36", "a37", "a38", "a39", "a40", "a41", "a42", "a43", "a44", "a45", "a46", "a47", "a48", "a49", "a50", "a51", "a52", "a53", "a54", "a55", "a56", "a57", "a58", "a59", "a60", "a61", "a62", "a63", "a64", "a65", "a66", "a67", "a68", "a69", "a70", "a71", "a72", "a73", "a74", "a75", "a76", "a77", "a78", "a79", "a80", "a81", "a82", "a83", "a84", "a85", "a86", "a87", "a88", "a89", "a90", "a91", "a92", "a93", "a94", "a95", "a96", "a97", "a98", "a99", "a100", "a101", "a102", "a103", "a104", "a105", "a106", "a107", "a108", "a109", "a110", "a111", "a112", "a113", "a114", "a115", "a116", "a117", "a118", "a119", "a120", "a121", "a122", "a123", "a124", "a125", "a126", "a127", "a128", "a129", "a130", "a131", "a132", "a133", "a134", "a135", "a136", "a137", "a138", "a139", "a140", "a141", "a142", "a143", "a144", "a145", "a146", "a147", "a148", "a149", "a150", "a151", "a152", "a153", "a154", "a155", "a156", "a157", "a158", "a159", "a160", "a161", "a162", "a163", "a164", "a165", "a166", "a167", "a168", "a169", "a170", "a171", "a172", "a173", "a174", "a175", "a176", "a177", "a178", "a179", "a180", "a181", "a182", "a183", "a184", "a185", "a186", "a187", "a188", "a189", "a190", "a191", "a192", "a193", "a194", "a195", "a196", "a197", "a198", "a199", "a200", "a201", "a202", "a203", "a204", "a205", "a206", "a207", "a208", "a209", "a210", "a211", "a212", "a213", "a214", "a215", "a216", "a217", "a218", "a219", "a220", "a221", "a222", "a223", "a224", "a225", "a226", "a227", "a228", "a229", "a230", "a231", "a232", "a233", "a234", "a235", "a236", "a237", "a238", "a239", "a240", "a241", "a242", "a243", "a244", "a245", "a246", "a247", "a248", "a249", "a250", "a251", "a252", "a253", "a254", "a255", "s48", "s49", "s50", "s51", "s52", "s53", "s54", "s55", "s56", "s57", "s58", "s59", "s60", "s65", "s66", "s67", "s81", "s82", "s83", "s95", "s99", "vcc", "scc", "memory");
    __builtin_amdgcn_endpgm();
    __builtin_unreachable();
}

__device__ __forceinline__ void u_phase(const bf16x16 (&wt)[7][7], lptr mt, lptr uimg, int aoff, int idx, int w, int l) {
    const int g = l >> 4, n = l & 15;
    bf16x8 mf[7];
#pragma unroll
    for (int k = 0; k < 7; ++k) mf[k] = afrag(mt, aoff, k);
#pragma unroll
    for (int j = 0; j < 7; ++j) {
        f32x4 acc = {0, 0, 0, 0};
#pragma unroll
        for (int k = 0; k < 7; ++k) acc = smf(mf[k], wt[j][k], acc, idx);
        const int v = 64 * j + 16 * w + n, hh = v / 28, c = v % 28, t2 = c >> 4, cc = c & 15;
        *(LAS unsigned*)(uimg + ((t2 * 16 + hh) * 16 + cc) * 16 + g * 4) = pk2(acc[0] + acc[1], acc[2] + acc[3]);
    }
}
__device__ __forceinline__ void load_bt_slices(bf16x16 (&wt)[7][7], rsrc_t rs, int jj, int w, int l) {
    LOAD_SPARSE_W(wt, rs, (unsigned)(OFF_BS + (size_t)jj * D * D * 2))
}
__device__ __forceinline__ void stage_vw(rsrc_t rs, int s, int jj, lptr uimg, int w, int l) {
    v4u v[8];
#pragma unroll
    for (int c = 0; c < 8; ++c) { int blk = w * 8 + c; bool pad = (blk >= 16) && ((l & 15) >= 12);
        v[c] = gld128(rs, pad ? OOB : (unsigned)(OFF_VW + (size_t)s * VW_SEG + (jj * 32 + blk) * 1024 + l * 16)); }
#pragma unroll
    for (int c = 0; c < 8; ++c) *(LAS v4u*)(uimg + (w * 8 + c) * 1024 + l * 16) = v[c];
}

constexpr int O_UM = 0, O_UX0 = 8192, O_MP = 73728, O_DUM = O_MP + 7168;
__device__ __forceinline__ void role_O(const Params& p, lptr smem, int jj, int w) {
    const int l = opaque_lane();
    const int g = l >> 4, n = l & 15, tid = w * 64 + l;
    for (int i = tid; i < 8192 / 16; i += 256) *(LAS v4u*)(smem + O_UM + i * 16) = v4u{0, 0, 0, 0};
    __syncthreads();
    const unsigned idx = (l & 1) ? 0xEEEE : 0x4444;
    const unsigned a2 = O_MP + afrag_off(l);
    const unsigned a3 = w * 2048 + l * 16;
    unsigned a4, a5;
    { int q = w * 128 + l; int j = q >> 5, c = q & 31; a4 = c < 28 ? O_MP + (28 * j + c) * 16 : O_DUM + l * 16; }
    { int q = w * 128 + 64 + l; int j = q >> 5, c = q & 31; a5 = c < 28 ? O_MP + (28 * j + c) * 16 : O_DUM + l * 16; }
    unsigned ua[7], ro[7];
#pragma unroll
    for (int j = 0; j < 7; ++j) {
        const int v = 64 * j + 16 * w + n, hh = v / 28, c = v % 28, t2 = c >> 4, cc = c & 15;
        ua[j] = O_UM + ((((t2 * 2 + (hh >> 3)) * 2 + ((hh >> 2) & 1)) * 64) + (2 * ((hh >> 1) & 1) + (g >> 1)) * 16 + cc) * 16 + (2 * (g & 1) + (hh & 1)) * 4;
        ro[j] = l * 16;
    }
    const unsigned a0 = (w & 1) * 1024 + (2 * g + (l & 1)) * 128 + ((l >> 1) & 7) * 16;
    unsigned a13 = O_UX0 + w * 4096 + l * 16;
    const unsigned a14 = (w >= 2 && n >= 12) ? OOB : l * 16;
    const unsigned a15 = O_UM + (w & 1) * 2048 + l * 16;
    const int hb = (n >> 1) & 1;
    const unsigned a16 = jj * 512 + (w & 1) * 256 + (2 * (n >> 2) + (n & 1)) * 32 + (2 * g + hb) * 4;
    const unsigned long long hbm = 0xCCCCCCCCCCCCCCCCull;
    const unsigned long long wsa = (unsigned long long)p.ws;
    unsigned hist = OFF_MH2;
    asm volatile(R"ASM(
s_mov_b32 s50, s45
buffer_load_dwordx4 a[0:3], v17, s[40:43], s50 offen offset:0
buffer_load_dwordx4 a[4:7], v17, s[40:43], s50 offen offset:1024
buffer_load_dwordx4 a[8:11], v17, s[40:43], s50 offen offset:2048
buffer_load_dwordx4 a[12:15], v17, s[40:43], s50 offen offset:3072
s_add_u32 s50, s50, 4096
buffer_load_dwordx4 a[16:19], v17, s[40:43], s50 offen offset:0
buffer_load_dwordx4 a[20:23], v17, s[40:43], s50 offen offset:1024
buffer_load_dwordx4 a[24:27], v17, s[40:43], s50 offen offset:2048
buffer_load_dwordx4 a[28:31], v17, s[40:43], s50 offen offset:3072
s_add_u32 s50, s50, 4096
buffer_load_dwordx4 a[32:35], v17, s[40:43], s50 offen offset:0
buffer_load_dwordx4 a[36:39], v17, s[40:43], s50 offen offset:1024
buffer_load_dwordx4 a[40:43], v17, s[40:43], s50 offen offset:2048
buffer_load_dwordx4 a[44:47], v17, s[40:43], s50 offen offset:3072
s_add_u32 s50, s50, 4096
buffer_load_dwordx4 a[48:51], v17, s[40:43], s50 offen offset:0
buffer_load_dwordx4 a[52:55], v17, s[40:43], s50 offen offset:1024
buffer_load_dwordx4 a[56:59], v17, s[40:43], s50 offen offset:2048
buffer_load_dwordx4 a[60:63], v17, s[40:43], s50 offen offset:3072
s_add_u32 s50, s50, 4096
buffer_load_dwordx4 a[64:67], v17, s[40:43], s50 offen offset:0
buffer_load_dwordx4 a[68:71], v17, s[40:43], s50 offen offset:1024
buffer_load_dwordx4 a[72:75], v17, s[40:43], s50 offen offset:2048
buffer_load_dwordx4 a[76:79], v17, s[40:43], s50 offen offset:3072
s_add_u32 s50, s50, 4096
buffer_load_dwordx4 a[80:83], v17, s[40:43], s50 offen offset:0
buffer_load_dwordx4 a[84:87], v17, s[40:43], s50 offen offset:1024
buffer_load_dwordx4 a[88:91], v17, s[40:43], s50 offen offset:2048
buffer_load_dwordx4 a[92:95], v17, s[40:43], s50 offen offset:3072
s_add_u32 s50, s50, 4096
buffer_load_dwordx4 a[96:99], v17, s[40:43], s50 offen offset:0
buffer_load_dwordx4 a[100:103], v17, s[40:43], s50 offen offset:1024
buffer_load_dwordx4 a[104:107], v17, s[40:43], s50 offen offset:2048
buffer_load_dwordx4 a[108:111], v17, s[40:43], s50 offen offset:3072
s_add_u32 s50, s50, 4096
buffer_load_dwordx4 a[112:115], v17, s[40:43], s50 offen offset:0
buffer_load_dwordx4 a[116:119], v17, s[40:43], s50 offen offset:1024
buffer_load_dwordx4 a[120:123], v17, s[40:43], s50 offen offset:2048
buffer_load_dwordx4 a[124:127], v17, s[40:43], s50 offen offset:3072
s_add_u32 s50, s50, 4096
buffer_load_dwordx4 a[128:131], v17, s[40:43], s50 offen offset:0
buffer_load_dwordx4 a[132:135], v17, s[40:43], s50 offen offset:1024
buffer_load_dwordx4 a[136:139], v17, s[40:43], s50 offen offset:2048
buffer_load_dwordx4 a[140:143], v17, s[40:43], s50 offen offset:3072
s_add_u32 s50, s50, 4096
buffer_load_dwordx4 a[144:147], v17, s[40:43], s50 offen offset:0
buffer_load_dwordx4 a[148:151], v17, s[40:43], s50 offen offset:1024
buffer_load_dwordx4 a[152:155], v17, s[40:43], s50 offen offset:2048
buffer_load_dwordx4 a[156:159], v17, s[40:43], s50 offen offset:3072
s_add_u32 s50, s50, 4096
buffer_load_dwordx4 a[160:163], v17, s[40:43], s50 offen offset:0
buffer_load_dwordx4 a[164:167], v17, s[40:43], s50 offen offset:1024
s_waitcnt vmcnt(28)
buffer_load_dwordx4 a[168:171], v17, s[40:43], s50 offen offset:2048
buffer_load_dwordx4 a[172:175], v17, s[40:43], s50 offen offset:3072
s_add_u32 s50, s50, 4096
buffer_load_dwordx4 a[176:179], v17, s[40:43], s50 offen offset:0
buffer_load_dwordx4 a[180:183], v17, s[40:43], s50 offen offset:1024
buffer_load_dwordx4 a[184:187], v17, s[40:43], s50 offen offset:2048
buffer_load_dwordx4 a[188:191], v17, s[40:43], s50 offen offset:3072
s_add_u32 s50, s50, 4096
buffer_load_dwordx4 a[192:195], v17, s[40:43], s50 offen offset:0
buffer_load_dwordx4 a[196:199], v17, s[40:43], s50 offen offset:1024
buffer_load_dwordx4 a[200:203], v17, s[40:43], s50 offen offset:2048
buffer_load_dwordx4 a[204:207], v17, s[40:43], s50 offen offset:3072
s_add_u32 s50, s50, 4096
buffer_load_dwordx4 a[208:211], v17, s[40:43], s50 offen offset:0
buffer_load_dwordx4 a[212:215], v17, s[40:43], s50 offen offset:1024
buffer_load_dwordx4 a[216:219], v17, s[40:43], s50 offen offset:2048
buffer_load_dwordx4 a[220:223], v17, s[40:43], s50 offen offset:3072
s_add_u32 s50, s50, 4096
s_waitcnt vmcnt(28)
buffer_load_dwordx4 a[224:227], v17, s[40:43], s50 offen offset:0
buffer_load_dwordx4 a[228:231], v17, s[40:43], s50 offen offset:1024
buffer_load_dwordx4 a[232:235], v17, s[40:43], s50 offen offset:2048
buffer_load_dwordx4 a[236:239], v17, s[40:43], s50 offen offset:3072
s_add_u32 s50, s50, 4096
buffer_load_dwordx4 a[240:243], v17, s[40:43], s50 offen offset:0
buffer_load_dwordx4 a[244:247], v17, s[40:43], s50 offen offset:1024
buffer_load_dwordx4 a[248:251], v17, s[40:43], s50 offen offset:2048
buffer_load_dwordx4 a[252:255], v17, s[40:43], s50 offen offset:3072
s_add_u32 s50, s50, 4096
buffer_load_dwordx4 v[120:123], v17, s[40:43], s50 offen offset:0
buffer_load_dwordx4 v[124:127], v17, s[40:43], s50 offen offset:1024
buffer_load_dwordx4 v[128:131], v17, s[40:43], s50 offen offset:2048
buffer_load_dwordx4 v[132:135], v17, s[40:43], s50 offen offset:3072
s_add_u32 s50, s50, 4096
buffer_load_dwordx4 v[136:139], v17, s[40:43], s50 offen offset:0
buffer_load_dwordx4 v[140:143], v17, s[40:43], s50 offen offset:1024
s_waitcnt vmcnt(28)
buffer_load_dwordx4 v[144:147], v17, s[40:43], s50 offen offset:2048
buffer_load_dwordx4 v[148:151], v17, s[40:43], s50 offen offset:3072
s_add_u32 s50, s50, 4096
buffer_load_dwordx4 v[152:155], v17, s[40:43], s50 offen offset:0
buffer_load_dwordx4 v[156:159], v17, s[40:43], s50 offen offset:1024
buffer_load_dwordx4 v[160:163], v17, s[40:43], s50 offen offset:2048
buffer_load_dwordx4 v[164:167], v17, s[40:43], s50 offen offset:3072
s_add_u32 s50, s50, 4096
buffer_load_dwordx4 v[168:171], v17, s[40:43], s50 offen offset:0
buffer_load_dwordx4 v[172:175], v17, s[40:43], s50 offen offset:1024
buffer_load_dwordx4 v[176:179], v17, s[40:43], s50 offen offset:2048
buffer_load_dwordx4 v[180:183], v17, s[40:43], s50 offen offset:3072
s_add_u32 s50, s50, 4096
buffer_load_dwordx4 v[184:187], v17, s[40:43], s50 offen offset:0
buffer_load_dwordx4 v[188:191], v17, s[40:43], s50 offen offset:1024
buffer_load_dwordx4 v[192:195], v17, s[40:43], s50 offen offset:2048
buffer_load_dwordx4 v[196:199], v17, s[40:43], s50 offen offset:3072
s_add_u32 s50, s50, 4096
s_waitcnt vmcnt(28)
buffer_load_dwordx4 v[200:203], v17, s[40:43], s50 offen offset:0
buffer_load_dwordx4 v[204:207], v17, s[40:43], s50 offen offset:1024
buffer_load_dwordx4 v[208:211], v17, s[40:43], s50 offen offset:2048
buffer_load_dwordx4 v[212:215], v17, s[40:43], s50 offen offset:3072
s_add_u32 s50, s50, 4096
buffer_load_dwordx4 v[216:219], v17, s[40:43], s50 offen offset:0
buffer_load_dwordx4 v[220:223], v17, s[40:43], s50 offen offset:1024
buffer_load_dwordx4 v[224:227], v17, s[40:43], s50 offen offset:2048
buffer_load_dwordx4 v[228:231], v17, s[40:43], s50 offen offset:3072
s_add_u32 s50, s50, 4096
buffer_load_dwordx4 v[232:235], v17, s[40:43], s50 offen offset:0
buffer_load_dwordx4 v[236:239], v17, s[40:43], s50 offen offset:1024
buffer_load_dwordx4 v[240:243], v17, s[40:43], s50 offen offset:2048
buffer_load_dwordx4 v[244:247], v17, s[40:43], s50 offen offset:3072
s_add_u32 s50, s50, 4096
buffer_load_dwordx4 v[248:251], v17, s[40:43], s50 offen offset:0
buffer_load_dwordx4 v[252:255], v17, s[40:43], s50 offen offset:1024
s_waitcnt vmcnt(28)
s_waitcnt vmcnt(0)
s_lshl_b32 s58, s44, 11
v_subrev_u32 v19, s58, v3
v_mov_b32 v17, 0
v_cmp_eq_u32 vcc, 0, v19
v_bfrev_b32 v22, 1
s_lshl_b32 s58, s44, 10
v_cndmask_b32 v22, v22, v17, vcc
s_add_u32 s58, s58, 81920
v_add_u32 v20, s58, v19
s_mov_b32 s65, 0
s_mov_b32 s54, s63
s_mov_b32 s81, s82
s_add_u32 s92, s63, s84
s_add_u32 s93, s82, s84
s_mov_b32 s57, 40000
s_mov_b32 s60, 0
s_mov_b32 s48, s47
s_mov_b32 s49, s61
s_mov_b32 s66, s62
s_mov_b32 s67, s46
s_mov_b32 s83, s80
v_mbcnt_lo_u32_b32 v24, -1, 0
v_min_u32 v24, 5, v24
v_lshlrev_b32 v24, 7, v24
s_add_u32 s50, s66, 1038336
buffer_load_dword v25, v24, s[40:43], s50 offen
s_waitcnt vmcnt(0)
v_readlane_b32 s52, v25, 0
v_readlane_b32 s53, v25, 1
s_cmp_lt_u32 s53, s52
s_cselect_b32 s58, 8192, 0
s_add_u32 s48, s48, s58
v_readlane_b32 s52, v25, 2
v_readlane_b32 s53, v25, 3
s_cmp_lt_u32 s53, s52
s_cselect_b32 s58, 8192, 0
s_add_u32 s49, s49, s58
v_readlane_b32 s52, v25, 4
v_readlane_b32 s53, v25, 5
s_cmp_lt_u32 s53, s52
s_cselect_b32 s58, 8192, 0
s_add_u32 s66, s66, s58
s_mov_b32 s56, 0
50:
buffer_load_dword v18, v17, s[40:43], s83 offen sc1
s_waitcnt vmcnt(0)
v_readfirstlane_b32 s58, v18
s_cmp_ge_u32 s58, 16
s_cbranch_scc1 51f
s_sleep 2
s_add_u32 s56, s56, 1
s_cmp_lt_u32 s56, s57
s_cbranch_scc1 50b
s_mov_b32 s57, 1
s_mov_b32 s60, 1
51:
s_add_u32 s96, s97, 8192
s_cmp_eq_u32 s44, 0
s_cbranch_scc1 53f
s_add_u32 m0, s96, 0
s_add_u32 s50, s67, 0
buffer_load_dwordx4 v14, s[40:43], s50 offen sc1 lds
s_add_u32 m0, s96, 1024
s_add_u32 s50, s67, 1024
buffer_load_dwordx4 v14, s[40:43], s50 offen sc1 lds
s_add_u32 m0, s96, 2048
s_add_u32 s50, s67, 2048
buffer_load_dwordx4 v14, s[40:43], s50 offen sc1 lds
s_add_u32 m0, s96, 3072
s_add_u32 s50, s67, 3072
buffer_load_dwordx4 v14, s[40:43], s50 offen sc1 lds
s_add_u32 m0, s96, 4096
s_add_u32 s50, s67, 4096
buffer_load_dwordx4 v14, s[40:43], s50 offen sc1 lds
s_add_u32 m0, s96, 5120
s_add_u32 s50, s67, 5120
buffer_load_dwordx4 v14, s[40:43], s50 offen sc1 lds
s_add_u32 m0, s96, 6144
s_add_u32 s50, s67, 6144
buffer_load_dwordx4 v14, s[40:43], s50 offen sc1 lds
s_add_u32 m0, s96, 7168
s_add_u32 s50, s67, 7168
buffer_load_dwordx4 v14, s[40:43], s50 offen sc1 lds
s_cmp_lg_u32 s44, 1
s_cbranch_scc1 53f
s_add_u32 m0, s96, 8192
s_add_u32 s50, s67, 8192
buffer_load_dwordx4 v14, s[40:43], s50 offen sc1 lds
s_add_u32 m0, s96, 9216
s_add_u32 s50, s67, 9216
buffer_load_dwordx4 v14, s[40:43], s50 offen sc1 lds
s_add_u32 m0, s96, 10240
s_add_u32 s50, s67, 10240
buffer_load_dwordx4 v14, s[40:43], s50 offen sc1 lds
s_add_u32 m0, s96, 11264
s_add_u32 s50, s67, 11264
buffer_load_dwordx4 v14, s[40:43], s50 offen sc1 lds
s_add_u32 m0, s96, 12288
s_add_u32 s50, s67, 12288
buffer_load_dwordx4 v14, s[40:43], s50 offen sc1 lds
s_add_u32 m0, s96, 13312
s_add_u32 s50, s67, 13312
buffer_load_dwordx4 v14, s[40:43], s50 offen sc1 lds
s_add_u32 m0, s96, 14336
s_add_u32 s50, s67, 14336
buffer_load_dwordx4 v14, s[40:43], s50 offen sc1 lds
s_add_u32 m0, s96, 15360
s_add_u32 s50, s67, 15360
buffer_load_dwordx4 v14, s[40:43], s50 offen sc1 lds
53:
s_add_u32 s67, s67, 524288
s_add_u32 s83, s83, 128
s_add_u32 s96, s97, 40960
s_mov_b32 s56, 0
s_memrealtime s[52:53]
s_waitcnt lgkmcnt(0)
s_mov_b32 s50, s52
s_mov_b32 s99, s52
7:
s_memrealtime s[52:53]
s_memtime s[58:59]
s_waitcnt lgkmcnt(0)
s_cmp_eq_u32 s50, s52
s_cbranch_scc1 7b
s_mov_b32 s50, s52
s_sub_u32 s53, s52, s99
s_mul_i32 s53, s53, 24
s_sub_u32 s53, s53, s58
s_cmp_eq_u32 s56, 0
s_cselect_b32 s95, s53, s95
s_sub_u32 s59, s53, s95
s_cmp_gt_i32 s59, 0
s_cselect_b32 s95, s53, s95
s_add_u32 s56, s56, 1
s_cmp_lt_u32 s56, 8
s_cbranch_scc1 7b
s_mul_i32 s99, s99, 24
s_add_u32 s95, s95, s99
s_branch 6f
1:
2:
s_memtime s[52:53]
s_waitcnt lgkmcnt(0)
s_sub_u32 s58, s52, s98
s_cmp_lt_u32 s58, s54
s_cbranch_scc1 2b
6:
v_mov_b64 v[52:53], 0
v_mov_b64 v[54:55], 0
v_mov_b64 v[56:57], 0
v_mov_b64 v[58:59], 0
v_mov_b64 v[60:61], 0
v_mov_b64 v[62:63], 0
s_mov_b32 s56, 0
buffer_load_dword v18, v17, s[40:43], s83 offen sc1
10:
buffer_load_dwordx4 v[24:27], v3, s[40:43], s48 offen sc1
buffer_load_dwordx4 v[28:31], v3, s[40:43], s48 offen offset:1024 sc1
s_waitcnt vmcnt(0)
ds_write_b128 v4, v[24:27]
ds_write_b128 v5, v[28:31]
v_max3_u32 v21, v24, v25, v26
v_max3_u32 v23, v27, v28, v29
v_max3_u32 v21, v21, v30, v31
v_max_u32 v21, v21, v23
v_cmp_eq_u32 vcc, -1, v21
s_cbranch_vccz 12f
s_add_u32 s56, s56, 1
s_cmp_lt_u32 s56, s57
s_cbranch_scc0 11f
s_sleep 1
s_waitcnt lgkmcnt(0)
s_branch 10b
11:
s_mov_b32 s57, 1
s_mov_b32 s60, 1
12:
s_cmp_lt_u32 s65, 3
s_cbranch_scc1 4f
s_cmp_eq_u32 s56, 0
s_cbranch_scc0 3f
s_sub_u32 s54, s54, s85
s_branch 4f
3:
s_add_u32 s54, s54, s94
s_min_i32 s54, s54, s92
4:
s_add_u32 s58, s65, 1
s_cmp_ge_u32 s58, s64
s_cbranch_scc1 14f
s_mov_b32 s56, 0
13:
v_readfirstlane_b32 s58, v18
s_cmp_ge_u32 s58, 16
s_cbranch_scc1 14f
s_sleep 2
buffer_load_dword v18, v17, s[40:43], s83 offen sc1
s_waitcnt vmcnt(0)
s_add_u32 s56, s56, 1
s_cmp_lt_u32 s56, s57
s_cbranch_scc1 13b
s_mov_b32 s57, 1
s_mov_b32 s60, 1
14:
s_waitcnt lgkmcnt(0)
s_barrier
ds_read_b64_tr_b16 v[24:25], v2 offset:0
ds_read_b64_tr_b16 v[26:27], v2 offset:256
ds_read_b64_tr_b16 v[28:29], v2 offset:1024
ds_read_b64_tr_b16 v[30:31], v2 offset:1280
ds_read_b64_tr_b16 v[32:33], v2 offset:2048
ds_read_b64_tr_b16 v[34:35], v2 offset:2304
ds_read_b64_tr_b16 v[36:37], v2 offset:3072
ds_read_b64_tr_b16 v[38:39], v2 offset:3328
ds_read_b64_tr_b16 v[40:41], v2 offset:4096
ds_read_b64_tr_b16 v[42:43], v2 offset:4352
ds_read_b64_tr_b16 v[44:45], v2 offset:5120
ds_read_b64_tr_b16 v[46:47], v2 offset:5376
ds_read_b64_tr_b16 v[48:49], v2 offset:6144
ds_read_b64_tr_b16 v[50:51], v2 offset:6400
v_mbcnt_lo_u32_b32 v64, -1, 0
v_and_b32 v64, 15, v64
v_lshlrev_b32 v64, 7, v64
s_cmp_eq_u32 s44, 0
s_cbranch_scc1 60f
s_cmp_eq_u32 s44, 1
s_cbranch_scc1 61f
s_waitcnt lgkmcnt(12)
v_smfmac_f32_16x16x64_bf16 v[52:55], v[24:27], a[0:7], v1
s_waitcnt lgkmcnt(10)
v_smfmac_f32_16x16x64_bf16 v[52:55], v[28:31], a[8:15], v1
s_waitcnt lgkmcnt(8)
v_smfmac_f32_16x16x64_bf16 v[52:55], v[32:35], a[16:23], v1
s_waitcnt lgkmcnt(6)
v_smfmac_f32_16x16x64_bf16 v[52:55], v[36:39], a[24:31], v1
s_waitcnt lgkmcnt(4)
v_smfmac_f32_16x16x64_bf16 v[52:55], v[40:43], a[32:39], v1
s_waitcnt lgkmcnt(2)
v_smfmac_f32_16x16x64_bf16 v[52:55], v[44:47], a[40:47], v1
s_waitcnt lgkmcnt(0)
v_smfmac_f32_16x16x64_bf16 v[52:55], v[48:51], a[48:55], v1
v_smfmac_f32_16x16x64_bf16 v[56:59], v[24:27], a[56:63], v1
s_add_u32 m0, s96, 0
s_add_u32 s50, s67, 0
buffer_load_dwordx4 v14, s[40:43], s50 offen sc1 lds
v_smfmac_f32_16x16x64_bf16 v[56:59], v[28:31], a[64:71], v1
v_add_f32 v21, v52, v53
v_smfmac_f32_16x16x64_bf16 v[56:59], v[32:35], a[72:79], v1
v_add_f32 v23, v54, v55
v_smfmac_f32_16x16x64_bf16 v[56:59], v[36:39], a[80:87], v1
v_cvt_pk_bf16_f32 v21, v21, v23
v_smfmac_f32_16x16x64_bf16 v[56:59], v[40:43], a[88:95], v1
s_add_u32 m0, s96, 1024
s_add_u32 s50, s67, 1024
buffer_load_dwordx4 v14, s[40:43], s50 offen sc1 lds
ds_write_b32 v6, v21
v_smfmac_f32_16x16x64_bf16 v[56:59], v[44:47], a[96:103], v1
v_mov_b64 v[52:53], 0
v_smfmac_f32_16x16x64_bf16 v[56:59], v[48:51], a[104:111], v1
v_mov_b64 v[54:55], 0
v_smfmac_f32_16x16x64_bf16 v[60:63], v[24:27], a[112:119], v1
s_add_u32 m0, s96, 2048
s_add_u32 s50, s67, 2048
buffer_load_dwordx4 v14, s[40:43], s50 offen sc1 lds
v_smfmac_f32_16x16x64_bf16 v[60:63], v[28:31], a[120:127], v1
v_add_f32 v21, v56, v57
v_smfmac_f32_16x16x64_bf16 v[60:63], v[32:35], a[128:135], v1
v_add_f32 v23, v58, v59
v_smfmac_f32_16x16x64_bf16 v[60:63], v[36:39], a[136:143], v1
v_cvt_pk_bf16_f32 v21, v21, v23
v_smfmac_f32_16x16x64_bf16 v[60:63], v[40:43], a[144:151], v1
s_add_u32 m0, s96, 3072
s_add_u32 s50, s67, 3072
buffer_load_dwordx4 v14, s[40:43], s50 offen sc1 lds
ds_write_b32 v7, v21
v_smfmac_f32_16x16x64_bf16 v[60:63], v[44:47], a[152:159], v1
v_mov_b64 v[56:57], 0
v_smfmac_f32_16x16x64_bf16 v[60:63], v[48:51], a[160:167], v1
v_mov_b64 v[58:59], 0
v_smfmac_f32_16x16x64_bf16 v[52:55], v[24:27], a[168:175], v1
s_add_u32 m0, s96, 4096
s_add_u32 s50, s67, 4096
buffer_load_dwordx4 v14, s[40:43], s50 offen sc1 lds
v_smfmac_f32_16x16x64_bf16 v[52:55], v[28:31], a[176:183], v1
v_add_f32 v21, v60, v61
v_smfmac_f32_16x16x64_bf16 v[52:55], v[32:35], a[184:191], v1
v_add_f32 v23, v62, v63
v_smfmac_f32_16x16x64_bf16 v[52:55], v[36:39], a[192:199], v1
v_cvt_pk_bf16_f32 v21, v21, v23
v_smfmac_f32_16x16x64_bf16 v[52:55], v[40:43], a[200:207], v1
s_add_u32 m0, s96, 5120
s_add_u32 s50, s67, 5120
buffer_load_dwordx4 v14, s[40:43], s50 offen sc1 lds
ds_write_b32 v8, v21
v_smfmac_f32_16x16x64_bf16 v[52:55], v[44:47], a[208:215], v1
v_mov_b64 v[60:61], 0
v_smfmac_f32_16x16x64_bf16 v[52:55], v[48:51], a[216:223], v1
v_mov_b64 v[62:63], 0
v_smfmac_f32_16x16x64_bf16 v[56:59], v[24:27], a[224:231], v1
s_add_u32 m0, s96, 6144
s_add_u32 s50, s67, 6144
buffer_load_dwordx4 v14, s[40:43], s50 offen sc1 lds
v_smfmac_f32_16x16x64_bf16 v[56:59], v[28:31], a[232:239], v1
v_add_f32 v21, v52, v53
v_smfmac_f32_16x16x64_bf16 v[56:59], v[32:35], a[240:247], v1
v_add_f32 v23, v54, v55
v_smfmac_f32_16x16x64_bf16 v[56:59], v[36:39], a[248:255], v1
v_cvt_pk_bf16_f32 v21, v21, v23
v_smfmac_f32_16x16x64_bf16 v[56:59], v[40:43], v[120:127], v1
s_add_u32 m0, s96, 7168
s_add_u32 s50, s67, 7168
buffer_load_dwordx4 v14, s[40:43], s50 offen sc1 lds
ds_write_b32 v9, v21
v_smfmac_f32_16x16x64_bf16 v[56:59], v[44:47], v[128:135], v1
v_mov_b64 v[52:53], 0
v_smfmac_f32_16x16x64_bf16 v[56:59], v[48:51], v[136:143], v1
v_mov_b64 v[54:55], 0
v_smfmac_f32_16x16x64_bf16 v[60:63], v[24:27], v[144:151], v1
ds_read_b128 v[72:75], v13 offset:0
v_smfmac_f32_16x16x64_bf16 v[60:63], v[28:31], v[152:159], v1
v_add_f32 v21, v56, v57
v_smfmac_f32_16x16x64_bf16 v[60:63], v[32:35], v[160:167], v1
v_add_f32 v23, v58, v59
v_smfmac_f32_16x16x64_bf16 v[60:63], v[36:39], v[168:175], v1
v_cvt_pk_bf16_f32 v21, v21, v23
v_smfmac_f32_16x16x64_bf16 v[60:63], v[40:43], v[176:183], v1
ds_write_b32 v10, v21
v_smfmac_f32_16x16x64_bf16 v[60:63], v[44:47], v[184:191], v1
ds_read_b128 v[76:79], v13 offset:1024
v_smfmac_f32_16x16x64_bf16 v[60:63], v[48:51], v[192:199], v1
ds_read_b128 v[80:83], v13 offset:16384
ds_read_b128 v[84:87], v13 offset:17408
v_smfmac_f32_16x16x64_bf16 v[52:55], v[24:27], v[200:207], v1
ds_read_b128 v[88:91], v13 offset:2048
v_smfmac_f32_16x16x64_bf16 v[52:55], v[28:31], v[208:215], v1
v_add_f32 v21, v60, v61
v_smfmac_f32_16x16x64_bf16 v[52:55], v[32:35], v[216:223], v1
v_add_f32 v23, v62, v63
v_smfmac_f32_16x16x64_bf16 v[52:55], v[36:39], v[224:231], v1
v_cvt_pk_bf16_f32 v21, v21, v23
v_smfmac_f32_16x16x64_bf16 v[52:55], v[40:43], v[232:239], v1
ds_write_b32 v11, v21
v_smfmac_f32_16x16x64_bf16 v[52:55], v[44:47], v[240:247], v1
ds_read_b128 v[92:95], v13 offset:3072
v_smfmac_f32_16x16x64_bf16 v[52:55], v[48:51], v[248:255], v1
ds_read_b128 v[96:99], v13 offset:18432
ds_read_b128 v[100:103], v13 offset:19456
s_nop 7
v_add_f32 v21, v52, v53
v_add_f32 v23, v54, v55
v_cvt_pk_bf16_f32 v21, v21, v23
ds_write_b32 v12, v21
s_branch 62f
60:
s_waitcnt lgkmcnt(12)
v_smfmac_f32_16x16x64_bf16 v[52:55], v[24:27], a[0:7], v1
s_waitcnt lgkmcnt(10)
v_smfmac_f32_16x16x64_bf16 v[52:55], v[28:31], a[8:15], v1
s_waitcnt lgkmcnt(8)
v_smfmac_f32_16x16x64_bf16 v[52:55], v[32:35], a[16:23], v1
s_waitcnt lgkmcnt(6)
v_smfmac_f32_16x16x64_bf16 v[52:55], v[36:39], a[24:31], v1
s_waitcnt lgkmcnt(4)
v_smfmac_f32_16x16x64_bf16 v[52:55], v[40:43], a[32:39], v1
s_waitcnt lgkmcnt(2)
v_smfmac_f32_16x16x64_bf16 v[52:55], v[44:47], a[40:47], v1
s_waitcnt lgkmcnt(0)
v_smfmac_f32_16x16x64_bf16 v[52:55], v[48:51], a[48:55], v1
v_smfmac_f32_16x16x64_bf16 v[56:59], v[24:27], a[56:63], v1
v_smfmac_f32_16x16x64_bf16 v[56:59], v[28:31], a[64:71], v1
v_add_f32 v21, v52, v53
v_smfmac_f32_16x16x64_bf16 v[56:59], v[32:35], a[72:79], v1
v_add_f32 v23, v54, v55
v_smfmac_f32_16x16x64_bf16 v[56:59], v[36:39], a[80:87], v1
v_cvt_pk_bf16_f32 v21, v21, v23
v_smfmac_f32_16x16x64_bf16 v[56:59], v[40:43], a[88:95], v1
ds_write_b32 v6, v21
v_smfmac_f32_16x16x64_bf16 v[56:59], v[44:47], a[96:103], v1
v_mov_b64 v[52:53], 0
v_smfmac_f32_16x16x64_bf16 v[56:59], v[48:51], a[104:111], v1
v_mov_b64 v[54:55], 0
v_smfmac_f32_16x16x64_bf16 v[60:63], v[24:27], a[112:119], v1
v_smfmac_f32_16x16x64_bf16 v[60:63], v[28:31], a[120:127], v1
v_add_f32 v21, v56, v57
v_smfmac_f32_16x16x64_bf16 v[60:63], v[32:35], a[128:135], v1
v_add_f32 v23, v58, v59
v_smfmac_f32_16x16x64_bf16 v[60:63], v[36:39], a[136:143], v1
v_cvt_pk_bf16_f32 v21, v21, v23
v_smfmac_f32_16x16x64_bf16 v[60:63], v[40:43], a[144:151], v1
ds_write_b32 v7, v21
v_smfmac_f32_16x16x64_bf16 v[60:63], v[44:47], a[152:159], v1
v_mov_b64 v[56:57], 0
v_smfmac_f32_16x16x64_bf16 v[60:63], v[48:51], a[160:167], v1
v_mov_b64 v[58:59], 0
v_smfmac_f32_16x16x64_bf16 v[52:55], v[24:27], a[168:175], v1
v_smfmac_f32_16x16x64_bf16 v[52:55], v[28:31], a[176:183], v1
v_add_f32 v21, v60, v61
v_smfmac_f32_16x16x64_bf16 v[52:55], v[32:35], a[184:191], v1
v_add_f32 v23, v62, v63
v_smfmac_f32_16x16x64_bf16 v[52:55], v[36:39], a[192:199], v1
v_cvt_pk_bf16_f32 v21, v21, v23
v_smfmac_f32_16x16x64_bf16 v[52:55], v[40:43], a[200:207], v1
s_add_u32 s99, s66, 4096
buffer_load_dword v65, v64, s[40:43], s99 offen sc1
ds_write_b32 v8, v21
v_smfmac_f32_16x16x64_bf16 v[52:55], v[44:47], a[208:215], v1
v_mov_b64 v[60:61], 0
v_smfmac_f32_16x16x64_bf16 v[52:55], v[48:51], a[216:223], v1
v_mov_b64 v[62:63], 0
v_smfmac_f32_16x16x64_bf16 v[56:59], v[24:27], a[224:231], v1
v_smfmac_f32_16x16x64_bf16 v[56:59], v[28:31], a[232:239], v1
v_add_f32 v21, v52, v53
v_smfmac_f32_16x16x64_bf16 v[56:59], v[32:35], a[240:247], v1
v_add_f32 v23, v54, v55
v_smfmac_f32_16x16x64_bf16 v[56:59], v[36:39], a[248:255], v1
v_cvt_pk_bf16_f32 v21, v21, v23
v_smfmac_f32_16x16x64_bf16 v[56:59], v[40:43], v[120:127], v1
ds_write_b32 v9, v21
v_smfmac_f32_16x16x64_bf16 v[56:59], v[44:47], v[128:135], v1
v_mov_b64 v[52:53], 0
v_smfmac_f32_16x16x64_bf16 v[56:59], v[48:51], v[136:143], v1
v_mov_b64 v[54:55], 0
v_smfmac_f32_16x16x64_bf16 v[60:63], v[24:27], v[144:151], v1
ds_read_b128 v[72:75], v13 offset:0
v_smfmac_f32_16x16x64_bf16 v[60:63], v[28:31], v[152:159], v1
v_add_f32 v21, v56, v57
v_smfmac_f32_16x16x64_bf16 v[60:63], v[32:35], v[160:167], v1
v_add_f32 v23, v58, v59
v_smfmac_f32_16x16x64_bf16 v[60:63], v[36:39], v[168:175], v1
v_cvt_pk_bf16_f32 v21, v21, v23
v_smfmac_f32_16x16x64_bf16 v[60:63], v[40:43], v[176:183], v1
ds_write_b32 v10, v21
v_smfmac_f32_16x16x64_bf16 v[60:63], v[44:47], v[184:191], v1
ds_read_b128 v[76:79], v13 offset:1024
v_smfmac_f32_16x16x64_bf16 v[60:63], v[48:51], v[192:199], v1
ds_read_b128 v[80:83], v13 offset:16384
ds_read_b128 v[84:87], v13 offset:17408
v_smfmac_f32_16x16x64_bf16 v[52:55], v[24:27], v[200:207], v1
ds_read_b128 v[88:91], v13 offset:2048
v_smfmac_f32_16x16x64_bf16 v[52:55], v[28:31], v[208:215], v1
v_add_f32 v21, v60, v61
v_smfmac_f32_16x16x64_bf16 v[52:55], v[32:35], v[216:223], v1
v_add_f32 v23, v62, v63
v_smfmac_f32_16x16x64_bf16 v[52:55], v[36:39], v[224:231], v1
v_cvt_pk_bf16_f32 v21, v21, v23
v_smfmac_f32_16x16x64_bf16 v[52:55], v[40:43], v[232:239], v1
ds_write_b32 v11, v21
v_smfmac_f32_16x16x64_bf16 v[52:55], v[44:47], v[240:247], v1
ds_read_b128 v[92:95], v13 offset:3072
v_smfmac_f32_16x16x64_bf16 v[52:55], v[48:51], v[248:255], v1
ds_read_b128 v[96:99], v13 offset:18432
ds_read_b128 v[100:103], v13 offset:19456
s_nop 7
v_add_f32 v21, v52, v53
v_add_f32 v23, v54, v55
v_cvt_pk_bf16_f32 v21, v21, v23
ds_write_b32 v12, v21
s_mov_b32 s56, 0
s_memtime s[52:53]
s_waitcnt vmcnt(0) lgkmcnt(0)
s_branch 133f
130:
buffer_load_dword v65, v64, s[40:43], s99 offen sc1
s_memtime s[52:53]
s_waitcnt vmcnt(0) lgkmcnt(0)
133:
v_cmp_eq_u32 vcc, -1, v65
s_cbranch_vccz 131f
s_add_u32 s56, s56, 1
s_cmp_lt_u32 s56, s57
s_cbranch_scc0 132f
s_sleep 3
s_branch 130b
132:
s_mov_b32 s57, 1
s_mov_b32 s60, 1
131:
s_add_u32 s58, s52, s95
v_subrev_u32 v65, s58, v65
s_nop 1
v_max_i32_dpp v65, v65, v65 quad_perm:[1,0,3,2] row_mask:0xf bank_mask:0xf
s_nop 1
v_max_i32_dpp v65, v65, v65 quad_perm:[2,3,0,1] row_mask:0xf bank_mask:0xf
s_nop 1
v_max_i32_dpp v65, v65, v65 row_half_mirror row_mask:0xf bank_mask:0xf
s_nop 1
v_max_i32_dpp v65, v65, v65 row_mirror row_mask:0xf bank_mask:0xf
s_nop 1
v_readfirstlane_b32 s55, v65
s_add_u32 s55, s55, s58
v_mov_b32 v64, s55
v_mov_b32 v65, 86016
ds_write_b32 v65, v64
s_branch 62f
61:
s_waitcnt lgkmcnt(12)
v_smfmac_f32_16x16x64_bf16 v[52:55], v[24:27], a[0:7], v1
s_waitcnt lgkmcnt(10)
v_smfmac_f32_16x16x64_bf16 v[52:55], v[28:31], a[8:15], v1
s_waitcnt lgkmcnt(8)
v_smfmac_f32_16x16x64_bf16 v[52:55], v[32:35], a[16:23], v1
s_waitcnt lgkmcnt(6)
v_smfmac_f32_16x16x64_bf16 v[52:55], v[36:39], a[24:31], v1
s_waitcnt lgkmcnt(4)
v_smfmac_f32_16x16x64_bf16 v[52:55], v[40:43], a[32:39], v1
s_waitcnt lgkmcnt(2)
v_smfmac_f32_16x16x64_bf16 v[52:55], v[44:47], a[40:47], v1
s_waitcnt lgkmcnt(0)
v_smfmac_f32_16x16x64_bf16 v[52:55], v[48:51], a[48:55], v1
v_smfmac_f32_16x16x64_bf16 v[56:59], v[24:27], a[56:63], v1
s_add_u32 m0, s96, 0
s_add_u32 s50, s67, 0
buffer_load_dwordx4 v14, s[40:43], s50 offen sc1 lds
v_smfmac_f32_16x16x64_bf16 v[56:59], v[28:31], a[64:71], v1
v_add_f32 v21, v52, v53
v_smfmac_f32_16x16x64_bf16 v[56:59], v[32:35], a[72:79], v1
s_add_u32 m0, s96, 2048
s_add_u32 s50, s67, 2048
buffer_load_dwordx4 v14, s[40:43], s50 offen sc1 lds
v_add_f32 v23, v54, v55
v_smfmac_f32_16x16x64_bf16 v[56:59], v[36:39], a[80:87], v1
v_cvt_pk_bf16_f32 v21, v21, v23
v_smfmac_f32_16x16x64_bf16 v[56:59], v[40:43], a[88:95], v1
s_add_u32 m0, s96, 1024
s_add_u32 s50, s67, 1024
buffer_load_dwordx4 v14, s[40:43], s50 offen sc1 lds
ds_write_b32 v6, v21
v_smfmac_f32_16x16x64_bf16 v[56:59], v[44:47], a[96:103], v1
v_mov_b64 v[52:53], 0
v_smfmac_f32_16x16x64_bf16 v[56:59], v[48:51], a[104:111], v1
s_add_u32 m0, s96, 3072
s_add_u32 s50, s67, 3072
buffer_load_dwordx4 v14, s[40:43], s50 offen sc1 lds
v_mov_b64 v[54:55], 0
v_smfmac_f32_16x16x64_bf16 v[60:63], v[24:27], a[112:119], v1
s_add_u32 m0, s96, 4096
s_add_u32 s50, s67, 4096
buffer_load_dwordx4 v14, s[40:43], s50 offen sc1 lds
v_smfmac_f32_16x16x64_bf16 v[60:63], v[28:31], a[120:127], v1
v_add_f32 v21, v56, v57
v_smfmac_f32_16x16x64_bf16 v[60:63], v[32:35], a[128:135], v1
s_add_u32 m0, s96, 6144
s_add_u32 s50, s67, 6144
buffer_load_dwordx4 v14, s[40:43], s50 offen sc1 lds
v_add_f32 v23, v58, v59
v_smfmac_f32_16x16x64_bf16 v[60:63], v[36:39], a[136:143], v1
v_cvt_pk_bf16_f32 v21, v21, v23
v_smfmac_f32_16x16x64_bf16 v[60:63], v[40:43], a[144:151], v1
s_add_u32 m0, s96, 5120
s_add_u32 s50, s67, 5120
buffer_load_dwordx4 v14, s[40:43], s50 offen sc1 lds
ds_write_b32 v7, v21
v_smfmac_f32_16x16x64_bf16 v[60:63], v[44:47], a[152:159], v1
v_mov_b64 v[56:57], 0
v_smfmac_f32_16x16x64_bf16 v[60:63], v[48:51], a[160:167], v1
s_add_u32 m0, s96, 7168
s_add_u32 s50, s67, 7168
buffer_load_dwordx4 v14, s[40:43], s50 offen sc1 lds
v_mov_b64 v[58:59], 0
v_smfmac_f32_16x16x64_bf16 v[52:55], v[24:27], a[168:175], v1
s_add_u32 m0, s96, 8192
s_add_u32 s50, s67, 8192
buffer_load_dwordx4 v14, s[40:43], s50 offen sc1 lds
v_smfmac_f32_16x16x64_bf16 v[52:55], v[28:31], a[176:183], v1
v_add_f32 v21, v60, v61
v_smfmac_f32_16x16x64_bf16 v[52:55], v[32:35], a[184:191], v1
s_add_u32 m0, s96, 10240
s_add_u32 s50, s67, 10240
buffer_load_dwordx4 v14, s[40:43], s50 offen sc1 lds
v_add_f32 v23, v62, v63
v_smfmac_f32_16x16x64_bf16 v[52:55], v[36:39], a[192:199], v1
v_cvt_pk_bf16_f32 v21, v21, v23
v_smfmac_f32_16x16x64_bf16 v[52:55], v[40:43], a[200:207], v1
s_add_u32 m0, s96, 9216
s_add_u32 s50, s67, 9216
buffer_load_dwordx4 v14, s[40:43], s50 offen sc1 lds
ds_write_b32 v8, v21
v_smfmac_f32_16x16x64_bf16 v[52:55], v[44:47], a[208:215], v1
v_mov_b64 v[60:61], 0
v_smfmac_f32_16x16x64_bf16 v[52:55], v[48:51], a[216:223], v1
s_add_u32 m0, s96, 11264
s_add_u32 s50, s67, 11264
buffer_load_dwordx4 v14, s[40:43], s50 offen sc1 lds
v_mov_b64 v[62:63], 0
v_smfmac_f32_16x16x64_bf16 v[56:59], v[24:27], a[224:231], v1
s_add_u32 m0, s96, 12288
s_add_u32 s50, s67, 12288
buffer_load_dwordx4 v14, s[40:43], s50 offen sc1 lds
v_smfmac_f32_16x16x64_bf16 v[56:59], v[28:31], a[232:239], v1
v_add_f32 v21, v52, v53
v_smfmac_f32_16x16x64_bf16 v[56:59], v[32:35], a[240:247], v1
s_add_u32 m0, s96, 14336
s_add_u32 s50, s67, 14336
buffer_load_dwordx4 v14, s[40:43], s50 offen sc1 lds
v_add_f32 v23, v54, v55
v_smfmac_f32_16x16x64_bf16 v[56:59], v[36:39], a[248:255], v1
v_cvt_pk_bf16_f32 v21, v21, v23
v_smfmac_f32_16x16x64_bf16 v[56:59], v[40:43], v[120:127], v1
s_add_u32 m0, s96, 13312
s_add_u32 s50, s67, 13312
buffer_load_dwordx4 v14, s[40:43], s50 offen sc1 lds
ds_write_b32 v9, v21
v_smfmac_f32_16x16x64_bf16 v[56:59], v[44:47], v[128:135], v1
v_mov_b64 v[52:53], 0
v_smfmac_f32_16x16x64_bf16 v[56:59], v[48:51], v[136:143], v1
s_add_u32 m0, s96, 15360
s_add_u32 s50, s67, 15360
buffer_load_dwordx4 v14, s[40:43], s50 offen sc1 lds
v_mov_b64 v[54:55], 0
v_smfmac_f32_16x16x64_bf16 v[60:63], v[24:27], v[144:151], v1
ds_read_b128 v[72:75], v13 offset:0
v_smfmac_f32_16x16x64_bf16 v[60:63], v[28:31], v[152:159], v1
v_add_f32 v21, v56, v57
v_smfmac_f32_16x16x64_bf16 v[60:63], v[32:35], v[160:167], v1
v_add_f32 v23, v58, v59
v_smfmac_f32_16x16x64_bf16 v[60:63], v[36:39], v[168:175], v1
v_cvt_pk_bf16_f32 v21, v21, v23
v_smfmac_f32_16x16x64_bf16 v[60:63], v[40:43], v[176:183], v1
ds_write_b32 v10, v21
v_smfmac_f32_16x16x64_bf16 v[60:63], v[44:47], v[184:191], v1
ds_read_b128 v[76:79], v13 offset:1024
v_smfmac_f32_16x16x64_bf16 v[60:63], v[48:51], v[192:199], v1
ds_read_b128 v[80:83], v13 offset:16384
ds_read_b128 v[84:87], v13 offset:17408
v_smfmac_f32_16x16x64_bf16 v[52:55], v[24:27], v[200:207], v1
ds_read_b128 v[88:91], v13 offset:2048
v_smfmac_f32_16x16x64_bf16 v[52:55], v[28:31], v[208:215], v1
v_add_f32 v21, v60, v61
v_smfmac_f32_16x16x64_bf16 v[52:55], v[32:35], v[216:223], v1
v_add_f32 v23, v62, v63
v_smfmac_f32_16x16x64_bf16 v[52:55], v[36:39], v[224:231], v1
v_cvt_pk_bf16_f32 v21, v21, v23
v_smfmac_f32_16x16x64_bf16 v[52:55], v[40:43], v[232:239], v1
ds_write_b32 v11, v21
v_smfmac_f32_16x16x64_bf16 v[52:55], v[44:47], v[240:247], v1
ds_read_b128 v[92:95], v13 offset:3072
v_smfmac_f32_16x16x64_bf16 v[52:55], v[48:51], v[248:255], v1
ds_read_b128 v[96:99], v13 offset:18432
ds_read_b128 v[100:103], v13 offset:19456
s_nop 7
v_add_f32 v21, v52, v53
v_add_f32 v23, v54, v55
v_cvt_pk_bf16_f32 v21, v21, v23
ds_write_b32 v12, v21
62:
s_waitcnt lgkmcnt(0)
s_barrier
s_cmp_ge_u32 s44, 2
s_cbranch_scc1 80f
v_mov_b32 v64, 86016
ds_read_b32 v65, v64
ds_read_b128 v[104:107], v15 offset:0
ds_read_b128 v[108:111], v15 offset:1024
ds_read_b128 v[112:115], v15 offset:4096
ds_read_b128 v[116:119], v15 offset:5120
s_waitcnt lgkmcnt(0)
v_readfirstlane_b32 s55, v65
v_mov_b64 v[64:65], 0
v_mov_b64 v[66:67], 0
v_mov_b64 v[68:69], 0
v_mov_b64 v[70:71], 0
s_add_u32 s58, s55, s81
s_sub_u32 s58, s58, s95
s_mov_b32 s56, 0
100:
s_memtime s[52:53]
s_add_u32 s56, s56, 1
s_waitcnt lgkmcnt(0)
s_sub_u32 s53, s52, s58
s_cmp_gt_u32 s56, 160
s_cselect_b32 s53, 0, s53
s_cmp_lt_i32 s53, 0
s_cbranch_scc1 100b
s_mov_b32 s56, 0
buffer_load_dwordx4 v[24:27], v3, s[40:43], s49 offen sc1
buffer_load_dwordx4 v[28:31], v3, s[40:43], s49 offen offset:1024 sc1
buffer_load_dwordx4 v[32:35], v0, s[40:43], s66 offen sc1
102:
s_waitcnt vmcnt(2)
v_smfmac_f32_16x16x64_bf16 v[64:67], v[24:27], v[72:79], v1
v_max3_u32 v19, v24, v25, v26
v_smfmac_f32_16x16x64_bf16 v[68:71], v[24:27], v[80:87], v1
v_max_u32 v19, v19, v27
s_waitcnt vmcnt(1)
v_smfmac_f32_16x16x64_bf16 v[64:67], v[28:31], v[88:95], v1
v_max3_u32 v19, v19, v28, v29
v_smfmac_f32_16x16x64_bf16 v[68:71], v[28:31], v[96:103], v1
v_max3_u32 v19, v19, v30, v31
s_waitcnt vmcnt(0)
v_smfmac_f32_16x16x64_bf16 v[64:67], v[32:35], v[104:111], v1
v_max3_u32 v19, v19, v32, v33
v_smfmac_f32_16x16x64_bf16 v[68:71], v[32:35], v[112:119], v1
v_max3_u32 v19, v19, v34, v35
v_cmp_eq_u32 vcc, -1, v19
s_cbranch_vccnz 103f
s_nop 4
v_add_f32 v64, v64, v65
v_add_f32 v65, v66, v67
v_add_f32 v66, v68, v69
v_add_f32 v67, v70, v71
ds_write_b128 v20, v[64:67]
s_branch 105f
103:
s_nop 4
v_mov_b64 v[64:65], 0
v_mov_b64 v[66:67], 0
v_mov_b64 v[68:69], 0
v_mov_b64 v[70:71], 0
104:
s_add_u32 s56, s56, 1
s_cmp_lt_u32 s56, s57
s_cbranch_scc0 106f
s_sleep 1
buffer_load_dwordx4 v[24:27], v3, s[40:43], s49 offen sc1
buffer_load_dwordx4 v[28:31], v3, s[40:43], s49 offen offset:1024 sc1
buffer_load_dwordx4 v[32:35], v0, s[40:43], s66 offen sc1
s_waitcnt vmcnt(0)
v_max3_u32 v19, v24, v25, v26
v_max3_u32 v23, v27, v28, v29
v_max3_u32 v19, v19, v30, v31
v_max3_u32 v23, v23, v32, v33
v_max3_u32 v19, v19, v34, v35
v_max_u32 v19, v19, v23
v_cmp_eq_u32 vcc, -1, v19
s_cbranch_vccnz 104b
s_branch 102b
106:
s_mov_b32 s57, 0
s_mov_b32 s60, 1
105:
s_branch 81f
80:
v_mov_b32 v64, 86016
ds_read_b32 v65, v64
s_waitcnt lgkmcnt(0)
v_readfirstlane_b32 s55, v65
v_mov_b64 v[64:65], 0
v_mov_b64 v[66:67], 0
v_mov_b64 v[68:69], 0
v_mov_b64 v[70:71], 0
s_add_u32 s58, s55, s81
s_sub_u32 s58, s58, s95
s_mov_b32 s56, 0
120:
s_memtime s[52:53]
s_add_u32 s56, s56, 1
s_waitcnt lgkmcnt(0)
s_sub_u32 s53, s52, s58
s_cmp_gt_u32 s56, 160
s_cselect_b32 s53, 0, s53
s_cmp_lt_i32 s53, 0
s_cbranch_scc1 120b
s_mov_b32 s56, 0
buffer_load_dwordx4 v[24:27], v3, s[40:43], s49 offen sc1
buffer_load_dwordx4 v[28:31], v3, s[40:43], s49 offen offset:1024 sc1
122:
s_waitcnt vmcnt(1)
v_smfmac_f32_16x16x64_bf16 v[64:67], v[24:27], v[72:79], v1
v_max3_u32 v19, v24, v25, v26
v_smfmac_f32_16x16x64_bf16 v[68:71], v[24:27], v[80:87], v1
v_max_u32 v19, v19, v27
s_waitcnt vmcnt(0)
v_smfmac_f32_16x16x64_bf16 v[64:67], v[28:31], v[88:95], v1
v_max3_u32 v19, v19, v28, v29
v_smfmac_f32_16x16x64_bf16 v[68:71], v[28:31], v[96:103], v1
v_max3_u32 v19, v19, v30, v31
v_cmp_eq_u32 vcc, -1, v19
s_cbranch_vccnz 123f
s_nop 4
v_add_f32 v64, v64, v65
v_add_f32 v65, v66, v67
v_add_f32 v66, v68, v69
v_add_f32 v67, v70, v71
ds_write_b128 v20, v[64:67]
s_branch 125f
123:
s_nop 4
v_mov_b64 v[64:65], 0
v_mov_b64 v[66:67], 0
v_mov_b64 v[68:69], 0
v_mov_b64 v[70:71], 0
124:
s_add_u32 s56, s56, 1
s_cmp_lt_u32 s56, s57
s_cbranch_scc0 126f
s_sleep 1
buffer_load_dwordx4 v[24:27], v3, s[40:43], s49 offen sc1
buffer_load_dwordx4 v[28:31], v3, s[40:43], s49 offen offset:1024 sc1
s_waitcnt vmcnt(0)
v_max3_u32 v19, v24, v25, v26
v_max3_u32 v23, v27, v28, v29
v_max3_u32 v19, v19, v30, v31
v_max_u32 v19, v19, v23
v_cmp_eq_u32 vcc, -1, v19
s_cbranch_vccnz 124b
s_branch 122b
126:
s_mov_b32 s57, 0
s_mov_b32 s60, 1
125:
81:
s_mul_i32 s58, s44, 1016
s_add_u32 s50, s48, 16384
v_subrev_u32 v21, s58, v20
s_add_u32 s51, s51, 8192
s_waitcnt lgkmcnt(0)
s_barrier
s_cmp_ge_u32 s44, 2
s_cbranch_scc1 39f
ds_read_b64 v[24:25], v21
ds_read_b64 v[26:27], v21 offset:1024
ds_read_b64 v[28:29], v21 offset:2048
ds_read_b64 v[30:31], v21 offset:3072
s_waitcnt lgkmcnt(2)
v_add_f32 v24, v24, v26
v_add_f32 v25, v25, v27
s_waitcnt lgkmcnt(0)
v_add_f32 v28, v28, v30
v_add_f32 v29, v29, v31
v_add_f32 v24, v24, v28
v_add_f32 v25, v25, v29
s_nop 1
v_mov_b32_dpp v26, v24 quad_perm:[2,3,0,1] row_mask:0xf bank_mask:0xf
v_mov_b32_dpp v27, v25 quad_perm:[2,3,0,1] row_mask:0xf bank_mask:0xf
s_nop 0
v_cvt_pk_bf16_f32 v24, v24, v26
v_cvt_pk_bf16_f32 v25, v27, v25
v_cndmask_b32 v24, v24, v25, s[90:91]
v_min_u32 v24, -2, v24
buffer_store_dword v24, v16, s[40:43], s50 offen sc1
buffer_store_dword v24, v16, s[40:43], s51 offen sc1
39:
s_barrier
s_memtime s[52:53]
s_waitcnt lgkmcnt(0)
s_mov_b32 s98, s52
s_cmp_lt_u32 s65, 3
s_cbranch_scc1 46f
s_cmp_eq_u32 s56, 0
s_cbranch_scc0 45f
s_sub_u32 s81, s81, s85
s_branch 46f
45:
s_add_u32 s81, s81, s94
s_min_i32 s81, s81, s93
46:
s_add_u32 s65, s65, 1
s_add_u32 s48, s48, 16384
s_add_u32 s49, s49, 16384
s_add_u32 s66, s66, 16384
s_add_u32 s67, s67, 524288
s_add_u32 s83, s83, 128
s_and_b32 s58, s65, 1
s_lshl_b32 s58, s58, 15
s_sub_u32 s96, 40960, s58
s_add_u32 s96, s96, s97
v_xor_b32 v13, 0x8000, v13
s_cmp_lt_u32 s65, s64
s_cbranch_scc1 1b
s_waitcnt vmcnt(0)
s_cmp_eq_u32 s60, 0
s_cbranch_scc1 99f
v_mov_b32 v24, 0x222
buffer_store_dword v24, v22, s[40:43], s69 offen sc1
s_waitcnt vmcnt(0)
99:
)ASM"
        : "+{v13}"(a13), "+{v17}"(ro[0]), "+{v18}"(ro[1]), "+{v19}"(ro[2]), "+{v20}"(ro[3]), "+{v21}"(ro[4]), "+{v22}"(ro[5]), "+{v23}"(ro[6]), "+{s51}"(hist)
        : "{v0}"(a0), "{v1}"(idx), "{v2}"(a2), "{v3}"(a3), "{v4}"(a4), "{v5}"(a5), "{v6}"(ua[0]), "{v7}"(ua[1]), "{v8}"(ua[2]), "{v9}"(ua[3]), "{v10}"(ua[4]), "{v11}"(ua[5]), "{v12}"(ua[6]),
          "{v14}"(a14), "{v15}"(a15), "{v16}"(a16),
          "{s40}"((unsigned)wsa), "{s41}"((unsigned)(wsa >> 32) & 0xffffu), "{s42}"(0x7fffffffu), "{s43}"(0x00020000u),
          "{s44}"(w), "{s45}"((unsigned)(OFF_BS + (size_t)jj * D * D * 2 + w * 98 * 1024)), "{s46}"((unsigned)(OFF_VW + jj * 32768 + (w < 2 ? 0 : w * 8192))), "{s97}"((unsigned)(w < 2 ? 0 : w * 8192)), "{s47}"((unsigned)OFF_WM),
          "{s61}"((unsigned)OFF_WPX), "{s62}"((unsigned)OFF_WPM), "{s63}"(p.dM), "{s64}"(NSEG - 1), "{s82}"(p.dP),
          "{s80}"((unsigned)(OFF_CNT + CNT_VW * 64 * 128)), "{s[90:91]}"(hbm), "{s84}"(p.fast), "{s85}"(p.slow), "{s94}"(p.up),
          "{s68}"((unsigned)(OFF_DBG + (64 + jj * 4 + w) * 64)), "{s69}"((unsigned)OFF_ERR)
        : "v24", "v25", "v26", "v27", "v28", "v29", "v30", "v31", "v32", "v33", "v34", "v35", "v36", "v37", "v38", "v39", "v40", "v41", "v42", "v43", "v44", "v45", "v46", "v47", "v48", "v49", "v50", "v51", "v52", "v53", "v54", "v55", "v56", "v57", "v58", "v59", "v60", "v61", "v62", "v63", "v64", "v65", "v66", "v67", "v68", "v69", "v70", "v71", "v72", "v73", "v74", "v75", "v76", "v77", "v78", "v79", "v80", "v81", "v82", "v83", "v84", "v85", "v86", "v87", "v88", "v89", "v90", "v91", "v92", "v93", "v94", "v95", "v96", "v97", "v98", "v99", "v100", "v101", "v102", "v103", "v104", "v105", "v106", "v107", "v108", "v109", "v110", "v111", "v112", "v113", "v114", "v115", "v116", "v117", "v118", "v119", "v120", "v121", "v122", "v123", "v124", "v125", "v126", "v127", "v128", "v129", "v130", "v131", "v132", "v133", "v134", "v135", "v136", "v137", "v138", "v139", "v140", "v141", "v142", "v143", "v144", "v145", "v146", "v147", "v148", "v149", "v150", "v151", "v152", "v153", "v154", "v155", "v156", "v157", "v158", "v159", "v160", "v161", "v162", "v163", "v164", "v165", "v166", "v167", "v168", "v169", "v170", "v171", "v172", "v173", "v174", "v175", "v176", "v177", "v178", "v179", "v180", "v181", "v182", "v183", "v184", "v185", "v186", "v187", "v188", "v189", "v190", "v191", "v192", "v193", "v194", "v195", "v196", "v197", "v198", "v199", "v200", "v201", "v202", "v203", "v204", "v205", "v206", "v207", "v208", "v209", "v210", "v211", "v212", "v213", "v214", "v215", "v216", "v217", "v218", "v219", "v220", "v221", "v222", "v223", "v224", "v225", "v226", "v227", "v228", "v229", "v230", "v231", "v232", "v233", "v234", "v235", "v236", "v237", "v238", "v239", "v240", "v241", "v242", "v243", "v244", "v245", "v246", "v247", "v248", "v249", "v250", "v251", "v252", "v253", "v254", "v255", "a0", "a1", "a2", "a3", "a4", "a5", "a6", "a7", "a8", "a9", "a10", "a11", "a12", "a13", "a14", "a15", "a16", "a17", "a18", "a19", "a20", "a21", "a22", "a23", "a24", "a25", "a26", "a27", "a28", "a29", "a30", "a31", "a32", "a33", "a34", "a35", "a36", "a37", "a38", "a39", "a40", "a41", "a42", "a43", "a44", "a45", "a46", "a47", "a48", "a49", "a50", "a51", "a52", "a53", "a54", "a55", "a56", "a57", "a58", "a59", "a60", "a61", "a62", "a63", "a64", "a65", "a66", "a67", "a68", "a69", "a70", "a71", "a72", "a73", "a74", "a75", "a76", "a77", "a78", "a79", "a80", "a81", "a82", "a83", "a84", "a85", "a86", "a87", "a88", "a89", "a90", "a91", "a92", "a93", "a94", "a95", "a96", "a97", "a98", "a99", "a100", "a101", "a102", "a103", "a104", "a105", "a106", "a107", "a108", "a109", "a110", "a111", "a112", "a113", "a114", "a115", "a116", "a117", "a118", "a119", "a120", "a121", "a122", "a123", "a124", "a125", "a126", "a127", "a128", "a129", "a130", "a131", "a132", "a133", "a134", "a135", "a136", "a137", "a138", "a139", "a140", "a141", "a142", "a143", "a144", "a145", "a146", "a147", "a148", "a149", "a150", "a151", "a152", "a153", "a154", "a155", "a156", "a157", "a158", "a159", "a160", "a161", "a162", "a163", "a164", "a165", "a166", "a167", "a168", "a169", "a170", "a171", "a172", "a173", "a174", "a175", "a176", "a177", "a178", "a179", "a180", "a181", "a182", "a183", "a184", "a185", "a186", "a187", "a188", "a189", "a190", "a191", "a192", "a193", "a194", "a195", "a196", "a197", "a198", "a199", "a200", "a201", "a202", "a203", "a204", "a205", "a206", "a207", "a208", "a209", "a210", "a211", "a212", "a213", "a214", "a215", "a216", "a217", "a218", "a219", "a220", "a221", "a222", "a223", "a224", "a225", "a226", "a227", "a228", "a229", "a230", "a231", "a232", "a233", "a234", "a235", "a236", "a237", "a238", "a239", "a240", "a241", "a242", "a243", "a244", "a245", "a246", "a247", "a248", "a249", "a250", "a251", "a252", "a253", "a254", "a255", "s48", "s49", "s50", "s52", "s53", "s54", "s55", "s56", "s57", "s58", "s59", "s60", "s65", "s66", "s67", "s81", "s83", "s92", "s93", "s95", "s96", "s98", "s99", "vcc", "scc", "memory");
    __builtin_amdgcn_endpgm();
    __builtin_unreachable();
}

#define ISTAMP(k) do {} while (0)
struct Rs4 { unsigned a, b, c, d; };
__device__ __forceinline__ void dma16(rsrc_t rs, unsigned voff, unsigned soff, unsigned lds) {
    unsigned keep;
    asm volatile("s_mov_b32 %0, m0\n\ts_mov_b32 m0, %4\n\ts_nop 0\n\tbuffer_load_dwordx4 %1, %2, %3 offen lds\n\ts_mov_b32 m0, %0" : "=&s"(keep) : "v"(voff), "s"(rs), "s"(soff), "s"(lds) : "memory");
}
__device__ __forceinline__ void dma16_sc1(rsrc_t rs, unsigned voff, unsigned soff, unsigned lds) {
    unsigned keep;
    asm volatile("s_mov_b32 %0, m0\n\ts_mov_b32 m0, %4\n\ts_nop 0\n\tbuffer_load_dwordx4 %1, %2, %3 offen sc1 lds\n\ts_mov_b32 m0, %0" : "=&s"(keep) : "v"(voff), "s"(rs), "s"(soff), "s"(lds) : "memory");
}
#define BARRIER() asm volatile("s_waitcnt lgkmcnt(0)\n\ts_barrier" ::: "memory")
#define VMWAIT(N) asm volatile("s_waitcnt vmcnt(" #N ")" ::: "memory")
__device__ __forceinline__ void x_src_offsets(unsigned (&xa)[7], int w, int l) {
#pragma unroll
    for (int c = 0; c < 7; ++c) { const int q = (7 * w + c) * 64 + l, row = q / 56, chp = q % 56, ch = (chp & ~7) | ((chp ^ xswz(row)) & 7); xa[c] = (row * 56 + ch) * 16; }
}
__device__ __forceinline__ void dma_x(rsrc_t rs, const unsigned (&xa)[7], int seg, unsigned ldsbase, int w) {
#pragma unroll
    for (int c = 0; c < 7; ++c) dma16(rs, xa[c], (unsigned)(OFF_XN + seg * (L * D * 2)), ldsbase + (7 * w + c) * 1024);
}

#define LOAD_DENSE_W(wf, rs, base)                                                                        \
    _Pragma("unroll") for (int j = 0; j < 7; ++j) _Pragma("unroll") for (int k = 0; k < 14; ++k)          \
        wf[j][k] = as8(gld128p(rs, (base) + ((w * 7 + j) * 14 + k) * 1024 + l * 16));

template <int F> __device__ __forceinline__ void mfma_wB(f32x4& acc, bf16x8 a, bf16x8 w) {
    constexpr int k = F % 14;
    if constexpr (F < 64) { if constexpr (k == 0) asm volatile("v_mfma_f32_16x16x32_bf16 %0, %1, %2, 0" : "=&v"(acc) : "v"(a), "a"(w)); else asm volatile("v_mfma_f32_16x16x32_bf16 %0, %1, %2, %0" : "+v"(acc) : "v"(a), "a"(w)); }
    else { if constexpr (k == 0) asm volatile("v_mfma_f32_16x16x32_bf16 %0, %1, %2, 0" : "=&v"(acc) : "v"(a), "v"(w)); else asm volatile("v_mfma_f32_16x16x32_bf16 %0, %1, %2, %0" : "+v"(acc) : "v"(a), "v"(w)); }
}
template <int F> __device__ __forceinline__ void mfma_wA(f32x4& acc, bf16x8 w, bf16x8 b) {
    constexpr int k = F % 14;
    if constexpr (F < 64) { if constexpr (k == 0) asm volatile("v_mfma_f32_16x16x32_bf16 %0, %1, %2, 0" : "=&v"(acc) : "a"(w), "v"(b)); else asm volatile("v_mfma_f32_16x16x32_bf16 %0, %1, %2, %0" : "+v"(acc) : "a"(w), "v"(b)); }
    else { if constexpr (k == 0) asm volatile("v_mfma_f32_16x16x32_bf16 %0, %1, %2, 0" : "=&v"(acc) : "v"(w), "v"(b)); else asm volatile("v_mfma_f32_16x16x32_bf16 %0, %1, %2, %0" : "+v"(acc) : "v"(w), "v"(b)); }
}
template <int K, bool WB> __device__ __forceinline__ void kstep(f32x4 (&acc)[7], bf16x8 x, const bf16x8 (&wf)[7][14]) {
    if constexpr (WB) { mfma_wB<0 * 14 + K>(acc[0], x, wf[0][K]); mfma_wB<1 * 14 + K>(acc[1], x, wf[1][K]); mfma_wB<2 * 14 + K>(acc[2], x, wf[2][K]); mfma_wB<3 * 14 + K>(acc[3], x, wf[3][K]);
        mfma_wB<4 * 14 + K>(acc[4], x, wf[4][K]); mfma_wB<5 * 14 + K>(acc[5], x, wf[5][K]); mfma_wB<6 * 14 + K>(acc[6], x, wf[6][K]); }
    else { mfma_wA<0 * 14 + K>(acc[0], wf[0][K], x); mfma_wA<1 * 14 + K>(acc[1], wf[1][K], x); mfma_wA<2 * 14 + K>(acc[2], wf[2][K], x); mfma_wA<3 * 14 + K>(acc[3], wf[3][K], x);
        mfma_wA<4 * 14 + K>(acc[4], wf[4][K], x); mfma_wA<5 * 14 + K>(acc[5], wf[5][K], x); mfma_wA<6 * 14 + K>(acc[6], wf[6][K], x); }
}
template <bool WB> __device__ __forceinline__ void gemm32(f32x4 (&acc)[7], lptr xe, lptr xo, const bf16x8 (&wf)[7][14]) {
    bf16x8 x[14];
#define LD(K) x[K] = as8(lds_r128(((K & 1) ? xo : xe) + 128 * (K >> 1)));
    LD(0) LD(1) LD(2) LD(3)
#define KS(K) if constexpr (K + 4 < 14) { LD((K + 4 < 14 ? K + 4 : 13)) } kstep<K, WB>(acc, x[K], wf);
    KS(0) KS(1) KS(2) KS(3) KS(4) KS(5) KS(6) KS(7) KS(8) KS(9) KS(10) KS(11) KS(12) KS(13)
#undef KS
#undef LD
    asm volatile("s_nop 7\n\ts_nop 3" : "+v"(acc[0]), "+v"(acc[1]), "+v"(acc[2]), "+v"(acc[3]), "+v"(acc[4]), "+v"(acc[5]), "+v"(acc[6]));
}

__device__ __forceinline__ void role_V(const Params& p, lptr smem, int h, int gr, int w, int l) {
    const int g = l >> 4, n = l & 15, tid = w * 64 + l;
    rsrc_t rs = mkrs(p.ws);
    unsigned xa[7]; x_src_offsets(xa, w, l);
    dma_x(rs, xa, gr, 0, w);
    bf16x8 wf[7][14];
    LOAD_DENSE_W(wf, rs, (unsigned)(OFF_BT + h * D * D * 2))
    const int x1 = 2 * (g & 1) + (g >> 1), sw = xswz(n);
    const int xeo = n * 896 + 16 * (x1 ^ sw), xoo = n * 896 + 16 * ((x1 + 4) ^ sw);
    unsigned so[7];
#pragma unroll
    for (int j = 0; j < 7; ++j) { const int c = 64 * j + 16 * w + n, jj = c / 28, cp = c % 28; so[j] = (((jj * 2 + (cp >> 4)) * 16 + h) * 64 + g * 16 + (cp & 15)) * 16; }
    VMWAIT(0);
    int buf = 0;
    for (int s = gr; s < NSEG; s += 2) {
        VMWAIT(7);
        BARRIER();
        dma_x(rs, xa, s + 2 < NSEG ? s + 2 : s, (buf ^ 1) * XI_BYTES, w);
        lptr xb = smem + buf * XI_BYTES;
        v2u lo[7];
        {
            f32x4 acc[7];
            gemm32<true>(acc, xb + xeo, xb + xoo, wf);
#pragma unroll
            for (int j = 0; j < 7; ++j) lo[j] = v2u{pk2(acc[j][0], acc[j][1]), pk2(acc[j][2], acc[j][3])};
        }
        if (s > gr + 2) { VMWAIT(7); BARRIER(); if (tid == 0) __hip_atomic_fetch_add(cntp(p.ws, CNT_VW, s - 2), 1u, RLX_AGENT); }
        {
            f32x4 acc[7];
            gemm32<true>(acc, xb + xeo + 16 * 896, xb + xoo + 16 * 896, wf);
#pragma unroll
            for (int j = 0; j < 7; ++j) gst128(rs, (unsigned)(OFF_VW + (size_t)s * VW_SEG) + so[j], v4u{lo[j].x, lo[j].y, pk2(acc[j][0], acc[j][1]), pk2(acc[j][2], acc[j][3])});
        }
        if (s == gr) { VMWAIT(0); BARRIER(); if (tid == 0) __hip_atomic_fetch_add(cntp(p.ws, CNT_VW, s), 1u, RLX_AGENT); }
        buf ^= 1;
    }
    VMWAIT(0); BARRIER();
    if (tid == 0) __hip_atomic_fetch_add(cntp(p.ws, CNT_VW, NSEG - 2 + gr), 1u, RLX_AGENT);
}

__device__ __forceinline__ void mfma_v(f32x4& acc, v4u a, v4u b) { asm volatile("s_nop 1\n\tv_mfma_f32_16x16x32_bf16 %0, %1, %2, %0" : "+v"(acc) : "v"(a), "v"(b)); }
__device__ __forceinline__ void mfma_v0(f32x4& acc, v4u a, v4u b) { asm volatile("s_nop 1\n\tv_mfma_f32_16x16x32_bf16 %0, %1, %2, 0" : "=&v"(acc) : "v"(a), "v"(b)); }
constexpr int T_MT = 2 * XI_BYTES, T_SX = T_MT + 7168, T_DUM = T_SX + 4 * 5 * 1024;
__device__ __forceinline__ void role_T(const Params& p, lptr smem, int h, int gr, int w, int l) {
    const int g = l >> 4, n = l & 15, tid = w * 64 + l;
    rsrc_t rs = mkrs(p.ws);
    unsigned xa[7]; x_src_offsets(xa, w, l);
    dma_x(rs, xa, gr, 0, w);
    bf16x8 wf[7][14];
    LOAD_DENSE_W(wf, rs, (unsigned)(OFF_AT + h * D * D * 2))
    const int x1 = 2 * (g & 1) + (g >> 1), sw = xswz(n);
    const int xeo = n * 896 + 16 * (x1 ^ sw), xoo = n * 896 + 16 * ((x1 + 4) ^ sw);
    const int kro = n * 896 + 16 * ((2 * w + (g >> 1)) ^ sw) + 8 * (g & 1);
    VMWAIT(0);
    int buf = 0;
    for (int s = gr; s < NSEG; s += 2) {
        VMWAIT(0);
        BARRIER();
        ISTAMP(0);
        dma_x(rs, xa, s + 2 < NSEG ? s + 2 : s, (buf ^ 1) * XI_BYTES, w);
        lptr xb = smem + buf * XI_BYTES;
        v2u tb[2][7];
        v4u ma, mb;
#pragma unroll
        for (int it = 0; it < 2; ++it) {
            f32x4 acc[7];
            if (it == 1) { const unsigned mo = (unsigned)(OFF_MH2 + s * 8192) + w * 2048 + l * 16; ma = gld128(rs, mo); mb = gld128(rs, mo + 1024); }
            gemm32<false>(acc, xb + xeo + it * 16 * 896, xb + xoo + it * 16 * 896, wf);
#pragma unroll
            for (int j = 0; j < 7; ++j) tb[it][j] = v2u{pk2(acc[j][0], acc[j][1]), pk2(acc[j][2], acc[j][3])};
        }
        f32x4 sp[5];
#pragma unroll
        for (int q = 0; q < 3; ++q) {
            const int kt = q == 2 ? 1 : 0, it = q == 0 ? 0 : 1;
            lptr kr = xb + kro + kt * 16 * 896;
#pragma unroll
            for (int pr = 0; pr < 3; ++pr) {
                const v2u a0 = lds_r64(kr + (2 * pr) * 128), a1 = lds_r64(kr + (2 * pr + 1) * 128);
                const v4u A = {a0.x, a0.y, a1.x, a1.y}, B = {tb[it][2 * pr].x, tb[it][2 * pr].y, tb[it][2 * pr + 1].x, tb[it][2 * pr + 1].y};
                if (pr == 0) mfma_v0(sp[q], A, B); else mfma_v(sp[q], A, B);
            }
            { const v2u a6 = lds_r64(kr + 6 * 128); mfma_v(sp[q], v4u{a6.x, a6.y, 0u, 0u}, v4u{tb[it][6].x, tb[it][6].y, 0u, 0u}); }
        }
        ISTAMP(1);
        gather_m(p.ws, rs, (unsigned)(OFF_MH2 + s * 8192), smem + T_MT, smem + T_DUM, w, l, 0x500 + s, ma, mb);
        BARRIER();
        ISTAMP(2);
        {
            lptr a = smem + T_MT + (2 * (4 * w + g)) * 32 + (n & 7) * 4;
            v2u mk[7];
#pragma unroll
            for (int t = 0; t < 7; ++t) { const unsigned d0 = *(LAS unsigned*)(a + t * 1024), d1 = *(LAS unsigned*)(a + t * 1024 + 32);
                mk[t] = v2u{__builtin_amdgcn_perm(d1, d0, 0x05040100u), __builtin_amdgcn_perm(d1, d0, 0x07060302u)}; }
#pragma unroll
            for (int it = 0; it < 2; ++it) {
#pragma unroll
                for (int pr = 0; pr < 3; ++pr) {
                    const v4u A = {mk[2 * pr].x, mk[2 * pr].y, mk[2 * pr + 1].x, mk[2 * pr + 1].y}, B = {tb[it][2 * pr].x, tb[it][2 * pr].y, tb[it][2 * pr + 1].x, tb[it][2 * pr + 1].y};
                    if (pr == 0) mfma_v0(sp[3 + it], A, B); else mfma_v(sp[3 + it], A, B);
                }
                mfma_v(sp[3 + it], v4u{mk[6].x, mk[6].y, 0u, 0u}, v4u{tb[it][6].x, tb[it][6].y, 0u, 0u});
            }
            asm volatile("s_nop 7\n\ts_nop 3" : "+v"(sp[0]), "+v"(sp[1]), "+v"(sp[2]), "+v"(sp[3]), "+v"(sp[4]));
        }
#pragma unroll
        for (int q = 0; q < 5; ++q) *(LAS f32x4*)(smem + T_SX + (w * 5 + q) * 1024 + l * 16) = sp[q];
        BARRIER();
        {
            const int i = w * 8 + (l >> 3), q8 = l & 7, gg = q8 >> 1, rt = q8 & 1, it = i >> 4, il = i & 15;
            f32x4 xs = {0, 0, 0, 0}; float xm = 0.f;
            const bool live = !(rt == 1 && it == 0);
            const int tile = rt == 0 ? it : 2;
#pragma unroll
            for (int ww = 0; ww < 4; ++ww) {
                xs += *(LAS f32x4*)(smem + T_SX + (ww * 5 + tile) * 1024 + (gg * 16 + il) * 16);
                xm += *(LAS float*)(smem + T_SX + (ww * 5 + 3 + it) * 1024 + ((q8 >> 2) * 16 + il) * 16 + (q8 & 3) * 4);
            }
            float x[4];
#pragma unroll
            for (int r = 0; r < 4; ++r) x[r] = (live && (16 * rt + 4 * gg + r) <= i) ? xs[r] : -INFINITY;
            float mx = dpp_max8(fmaxf(fmaxf(fmaxf(x[0], x[1]), fmaxf(x[2], x[3])), xm));
            float e[4];
#pragma unroll
            for (int r = 0; r < 4; ++r) e[r] = __builtin_amdgcn_exp2f(x[r] - mx);
            float em = __builtin_amdgcn_exp2f(xm - mx);
            float sum = dpp_sum8(e[0] + e[1] + e[2] + e[3] + em);
            float rc = __builtin_amdgcn_rcpf(sum);
            const unsigned pb = OFF_PX + s * PX_SEG;
            gst64(rs, pb + ((h * 2 + it) * 64 + gg * 16 + il) * 16 + rt * 8, v2u{san(pk2(e[0] * rc, e[1] * rc)), san(pk2(e[2] * rc, e[3] * rc))});
            float pm = em * rc, pn = dpp_xor1(pm);
            gst32(rs, (q8 & 1) ? OOB : pb + 32768 + (((h >> 2) * 2 + it) * 64 + (h & 3) * 16 + il) * 16 + (q8 >> 1) * 4, san(pk2(pm, pn)));
        }
        VMWAIT(0); BARRIER();
        if (tid == 0) __hip_atomic_fetch_add(cntp(p.ws, CNT_PX, s), 1u, RLX_AGENT);
        ISTAMP(3);
        buf ^= 1;
    }
}

constexpr int NG_XO = 3;
constexpr int XO_UX = 0, XO_UM = 32768, XO_MT = XO_UM + 8192, XO_DUM = XO_MT + 7168, XO_RED = XO_DUM + 1024;
template <int KH>
__device__ __forceinline__ void xo_tail(const Params& p, rsrc_t rs, lptr smem, int s, int jj, int it, int l) {
    const int g = l >> 4, n = l & 15;
    const unsigned pb = OFF_PX + s * PX_SEG + l * 16;
    const int row = s * L + 16 * it + n, cp = 16 * KH + 4 * g;
    v2u xv = {0, 0};
    if (cp < 28) xv = *(const v2u*)(p.x + (size_t)row * D + 28 * jj + cp);
    VMWAIT(0);
    __syncthreads();
    auto uaddr = [&](int t2, int k) -> lptr { return k < 8 ? smem + XO_UX + (t2 * 16 + KH * 8 + k) * 1024 + l * 16 : smem + XO_UM + (t2 * 16 + 4 * (KH * 2 + k - 8)) * 256 + l * 16; };
    v4u ua[5], ub[5];
#pragma unroll
    for (int k = 0; k < 5; ++k) ua[k] = lds_r128(uaddr(0, k));
    asm volatile("" : "+v"(ua[0]), "+v"(ua[1]), "+v"(ua[2]), "+v"(ua[3]), "+v"(ua[4]));
    if (s < DIRECT0) wait_cnt(p.ws, CNT_PX, s, 16, 0x800 + s);
    v4u pf[10];
    for (unsigned sp = 0;; ++sp) {
        asm volatile("" ::: "memory");
#pragma unroll
        for (int k = 0; k < 8; ++k) pf[k] = gld128(rs, pb + ((KH * 8 + k) * 2 + it) * 1024);
#pragma unroll
        for (int k = 0; k < 2; ++k) pf[8 + k] = gld128(rs, pb + 32768 + ((KH * 2 + k) * 2 + it) * 1024);
        if (s < DIRECT0) break;
        unsigned m = 0;
#pragma unroll
        for (int k = 0; k < 10; ++k) m = mx4(m, pf[k]);
        if (!anyS(m)) break;
        __builtin_amdgcn_s_sleep(1);
        if (sp > SPIN_CAP) { set_err(p.ws, 0xB00 + s); break; }
    }
    f32x4 a0 = {0, 0, 0, 0}, a1 = {0, 0, 0, 0};
#define XO_LD(dst, t2, k0) { _Pragma("unroll") for (int k = 0; k < 5; ++k) dst[k] = lds_r128(uaddr(t2, k0 + k)); asm volatile("" : "+v"(dst[0]), "+v"(dst[1]), "+v"(dst[2]), "+v"(dst[3]), "+v"(dst[4])); }
#define XO_MM(acc, src, k0) { _Pragma("unroll") for (int k = 0; k < 5; ++k) acc = mfma32(as8(src[k]), as8(pf[k0 + k]), acc); }
    XO_LD(ub, 0, 5) XO_MM(a0, ua, 0) XO_LD(ua, 1, 0) XO_MM(a0, ub, 5) XO_LD(ub, 1, 5) XO_MM(a1, ua, 0) XO_MM(a1, ub, 5)
#undef XO_LD
#undef XO_MM
    *(LAS f32x4*)(smem + XO_RED + (it * 2 + KH) * 1024 + l * 16) = KH ? a0 : a1;
    __syncthreads();
    const f32x4 a = (KH ? a1 : a0) + *(LAS f32x4*)(smem + XO_RED + (it * 2 + (KH ^ 1)) * 1024 + l * 16);
    gst64(rs, cp < 28 ? (unsigned)(OFF_HB + ((size_t)row * D + 28 * jj + cp) * 2) : OOB, v2u{san(pk2(bflo(xv.x) + a[0], bfhi(xv.x) + a[1])), san(pk2(bflo(xv.y) + a[2], bfhi(xv.y) + a[3]))});
}
__device__ __forceinline__ void role_XO(const Params& p, lptr smem, int jj, int gr, int w, int l) {
    const int n = l & 15, tid = w * 64 + l;
    rsrc_t rs = mkrs(p.ws);
    bf16x16 wt[7][7];
    load_bt_slices(wt, rs, jj, w, l);
    const int idx = (l & 1) ? 0xEEEE : 0x4444;
    const int aoff = afrag_off(l);
    for (int i = tid; i < 8192 / 16; i += 256) *(LAS v4u*)(smem + XO_UM + i * 16) = v4u{0, 0, 0, 0};
    for (int s = gr; s < NSEG; s += NG_XO) {
        ISTAMP(0);
        wait_cnt(p.ws, CNT_VW, s, 16, 0x700 + s);
        __syncthreads();
        const unsigned vo = (w >= 2 && n >= 12) ? OOB : (unsigned)(l * 16);
#pragma unroll
        for (int c = 0; c < 8; ++c) dma16_sc1(rs, vo, (unsigned)(OFF_VW + (size_t)s * VW_SEG + (jj * 32 + w * 8 + c) * 1024), XO_UX + (w * 8 + c) * 1024);
        { const unsigned mo = (unsigned)(OFF_MH2 + s * 8192) + w * 2048 + l * 16; gather_m(p.ws, rs, (unsigned)(OFF_MH2 + s * 8192), smem + XO_MT, smem + XO_DUM, w, l, 0x600 + s, gld128(rs, mo), gld128(rs, mo + 1024)); }
        __syncthreads();
        ISTAMP(1);
        u_phase(wt, smem + XO_MT, smem + XO_UM, aoff, idx, w, l);
        ISTAMP(2);
        if (w >> 1) xo_tail<1>(p, rs, smem, s, jj, w & 1, l); else xo_tail<0>(p, rs, smem, s, jj, w & 1, l);
        signal_cnt(p.ws, CNT_H, s);
        ISTAMP(3);
    }
}

__device__ __forceinline__ float xsum_g(float v) {
    auto a = __builtin_amdgcn_permlane16_swap(asu(v), asu(v), false, false); v = asf(a[0]) + asf(a[1]);
    auto b = __builtin_amdgcn_permlane32_swap(asu(v), asu(v), false, false); return asf(b[0]) + asf(b[1]);
}
__device__ __forceinline__ void role_F1(const Params& p, lptr smem, int f, int gr, int w, int l) {
    const int g = l >> 4, n = l & 15, tid = w * 64 + l;
    const int it = w & 1, ch = w >> 1;
    rsrc_t rs = mkrs(p.ws);
    constexpr int WS = 264;
    for (int c = tid; c < 448 * 16; c += 256) { int r = c >> 4, q = c & 15; v4u v = *(const v4u*)(p.w1 + (size_t)r * F + 128 * f + q * 8);
        *(LAS v2u*)(smem + r * WS + q * 16) = v2u{v.x, v.y}; *(LAS v2u*)(smem + r * WS + q * 16 + 8) = v2u{v.z, v.w}; }
    __syncthreads();
    bf16x8 wf[4][14];
    float bias[4][4], csum[4][4];
#pragma unroll
    for (int t = 0; t < 4; ++t) {
        f32x4 cs = {0, 0, 0, 0};
        const bf16x8 ones = as8(v4u{0x3f803f80u, 0x3f803f80u, 0x3f803f80u, 0x3f803f80u});
#pragma unroll
        for (int k = 0; k < 14; ++k) { lptr a = smem + (32 * k + 8 * g + (n >> 2)) * WS + (16 * (4 * ch + t) + 4 * (n & 3)) * 2; wf[t][k] = cat4(lds_tr(a), lds_tr(a + 4 * WS)); cs = mfma32(wf[t][k], ones, cs); }
#pragma unroll
        for (int r = 0; r < 4; ++r) { bias[t][r] = bf2f(p.b1[128 * f + 16 * (4 * ch + t) + 4 * g + r]); csum[t][r] = cs[r]; }
    }
    __syncthreads();
    constexpr int F1_ST = 32 * XSTR;
    for (int s = gr; s < NSEG; s += 2) {
        ISTAMP(0);
        if (s < DIRECT0) wait_cnt(p.ws, CNT_H, s, 16, 0x900 + s);
        ISTAMP(1);
        const int srow = tid >> 3, sub = tid & 7;
        v4u v[7];
        for (unsigned sp = 0;; ++sp) {
            asm volatile("" ::: "memory");
#pragma unroll
            for (int c = 0; c < 7; ++c) v[c] = gld128(rs, (unsigned)(OFF_HB + ((size_t)(s * L + srow) * D + (c * 8 + sub) * 8) * 2));
            if (s < DIRECT0) break;
            unsigned m = 0;
#pragma unroll
            for (int c = 0; c < 7; ++c) m = mx4(m, v[c]);
            if (!anyS(m)) break;
            __builtin_amdgcn_s_sleep(1);
            if (sp > SPIN_CAP) { set_err(p.ws, 0xC00 + s); break; }
        }
        __syncthreads();
#pragma unroll
        for (int c = 0; c < 7; ++c) *(LAS v4u*)(smem + srow * XSTR + (c * 8 + sub) * 16) = v[c];
        __syncthreads();
        f32x4 acc[4] = {f32x4{0, 0, 0, 0}, f32x4{0, 0, 0, 0}, f32x4{0, 0, 0, 0}, f32x4{0, 0, 0, 0}};
        v4u bfr[14];
#pragma unroll
        for (int k = 0; k < 14; ++k) bfr[k] = lds_r128(smem + (16 * it + n) * XSTR + (32 * k + 8 * g) * 2);
        const float cs0 = dpp_max8(bflo(v[0].x));
        float sm = 0.f, sq = 0.f;
#pragma unroll
        for (int k = 0; k < 14; ++k) {
#pragma unroll
            for (int t = 0; t < 4; ++t) acc[t] = mfma32(wf[t][k], as8(bfr[k]), acc[t]);
            if (k & 1) {
#pragma unroll
                for (int e = 0; e < 4; ++e) { const float a = bflo(v[k >> 1][e]) - cs0, bb = bfhi(v[k >> 1][e]) - cs0; sm += a + bb; sq = fmaf(a, a, fmaf(bb, bb, sq)); }
            }
        }
        sm = dpp_sum8(sm) * (1.0f / D); sq = dpp_sum8(sq) * (1.0f / D);
        if (sub == 0) *(LAS f32x2*)(smem + F1_ST + srow * 8) = f32x2{cs0 + sm, 1.0f / sqrtf(fmaxf(sq - sm * sm, 0.f) + 1e-5f)};
        __syncthreads();
        const f32x2 stt = *(LAS f32x2*)(smem + F1_ST + (16 * it + n) * 8);
        const float mean = stt[0], rsd = stt[1];
        ISTAMP(2);
#pragma unroll
        for (int t = 0; t < 4; ++t) {
            float y[4];
#pragma unroll
            for (int r = 0; r < 4; ++r) { const float u = fmaxf(fmaf(rsd, acc[t][r] - mean * csum[t][r], bias[t][r]), 0.f); y[r] = u * u; }
            { const int T = 8 * f + 4 * ch + t;
              gst64(rs, (unsigned)(OFF_HID + (size_t)s * (L * F * 2) + ((it * 56 + (T >> 1)) * 64 + (2 * (T & 1) + (g >> 1)) * 16 + n) * 16 + (g & 1) * 8), v2u{san(pk2(y[0], y[1])), san(pk2(y[2], y[3]))}); }
        }
        signal_cnt(p.ws, CNT_HID, s);
        ISTAMP(3);
    }
}

__device__ __forceinline__ void role_F2(const Params& p, lptr smem, int cp, int it, int gr, int w, int l) {
    const int g = l >> 4, n = l & 15, tid = w * 64 + l;
    rsrc_t rs = mkrs(p.ws);
    constexpr int WS = 72;
    for (int c = tid; c < 1792 * 4; c += 256) { int r = c >> 2, q = c & 3; v4u v = *(const v4u*)(p.w2 + (size_t)r * D + 32 * cp + q * 8);
        *(LAS v2u*)(smem + r * WS + q * 16) = v2u{v.x, v.y}; *(LAS v2u*)(smem + r * WS + q * 16 + 8) = v2u{v.z, v.w}; }
    __syncthreads();
    bf16x8 wf[2][14];
#pragma unroll
    for (int t = 0; t < 2; ++t)
#pragma unroll
        for (int k = 0; k < 14; ++k) { lptr a = smem + (448 * w + 32 * k + 8 * g + (n >> 2)) * WS + (16 * t + 4 * (n & 3)) * 2; wf[t][k] = cat4(lds_tr(a), lds_tr(a + 4 * WS)); }
    __syncthreads();
    const int col = 32 * cp + 16 * (w & 1) + 4 * g;
    const v2u bv = *(const v2u*)(p.b2 + col);
    for (int s = gr; s < NSEG; s += 2) {
        ISTAMP(0);
        if (s < DIRECT0) wait_cnt(p.ws, CNT_HID, s, 14, 0xA00 + s);
        ISTAMP(1);
        f32x4 acc[2] = {f32x4{0, 0, 0, 0}, f32x4{0, 0, 0, 0}};
        const int row = s * L + 16 * it + n;
        const unsigned hb = (unsigned)(OFF_HID + (size_t)s * (L * F * 2) + ((it * 56 + 14 * w) * 64 + l) * 16);
        v4u hf[14]; v2u hv;
        for (unsigned sp = 0;; ++sp) {
            asm volatile("" ::: "memory");
#pragma unroll
            for (int k = 0; k < 14; ++k) hf[k] = gld128(rs, hb + k * 1024);
            hv = gld64(rs, (unsigned)(OFF_HB + ((size_t)row * D + col) * 2));
            if (s < DIRECT0) break;
            unsigned m = umax(hv.x, hv.y);
#pragma unroll
            for (int k = 0; k < 14; ++k) m = mx4(m, hf[k]);
            if (!anyS(m)) break;
            __builtin_amdgcn_s_sleep(1);
            if (sp > SPIN_CAP) { set_err(p.ws, 0xD00 + s); break; }
        }
#pragma unroll
        for (int k = 0; k < 14; ++k) { acc[0] = mfma32(wf[0][k], as8(hf[k]), acc[0]); acc[1] = mfma32(wf[1][k], as8(hf[k]), acc[1]); }
        __syncthreads();
        *(LAS f32x4*)(smem + (w * 2 + 0) * 1024 + l * 16) = acc[0];
        *(LAS f32x4*)(smem + (w * 2 + 1) * 1024 + l * 16) = acc[1];
        __syncthreads();
        if (w < 2) {
            f32x4 a = *(LAS f32x4*)(smem + (0 * 2 + w) * 1024 + l * 16) + *(LAS f32x4*)(smem + (1 * 2 + w) * 1024 + l * 16) + *(LAS f32x4*)(smem + (2 * 2 + w) * 1024 + l * 16) + *(LAS f32x4*)(smem + (3 * 2 + w) * 1024 + l * 16);
            v2u o = {pk2(a[0] + bflo(bv.x) + bflo(hv.x), a[1] + bfhi(bv.x) + bfhi(hv.x)), pk2(a[2] + bflo(bv.y) + bflo(hv.y), a[3] + bfhi(bv.y) + bfhi(hv.y))};
            *(v2u*)(p.out + (size_t)row * D + col) = o;
        }
        ISTAMP(3);
    }
}

__global__ void __launch_bounds__(256, 1) k1(Params p) {
    extern __shared__ __attribute__((aligned(16))) char smem_[];
    lptr smem = (lptr)smem_;
    const int bid = blockIdx.x, tid = threadIdx.x, l = tid & 63, w = __builtin_amdgcn_readfirstlane(tid >> 6);
    const int x = bid & 7, y = bid >> 3;
#define ENDSTAMP() do {} while (0)
    if (x == 0) {
        if (y < 16) role_S(p, y, w); else role_O(p, smem, y - 16, w);
        return;
    }
    if (!(y < 10 && (x - 1) + 7 * y < 32)) { const unsigned long long t0 = __builtin_amdgcn_s_memrealtime(); while (__builtin_amdgcn_s_memrealtime() - t0 < (unsigned long long)p.hold) __builtin_amdgcn_s_sleep(8); }
    if (y < 10) {
        const int r = (x - 1) + 7 * y;
        if (r < 32) role_V(p, smem, r & 15, r >> 4, w, l);
        else if (r < 64) role_T(p, smem, r & 15, (r - 32) >> 4, w, l);
    } else {
        const int z = y - 10;
        if (x <= 3) { if (z < 16) role_XO(p, smem, z, x - 1, w, l); }
        else if (x == 4) { if (z < 14) role_F1(p, smem, z, 0, w, l); }
        else if (x == 5) { if (z < 14) role_F1(p, smem, z, 1, w, l); }
    }
    if (y >= 10) {
        const int z = y - 10; int q = -1, gr = 0;
        if (x == 6) { q = z; gr = 0; } else if (x == 7) { q = z; gr = 1; }
        else if (x == 4 && z >= 14 && z < 20) { q = 22 + z - 14; gr = 0; } else if (x == 5 && z >= 14 && z < 20) { q = 22 + z - 14; gr = 1; }
        if (q >= 0) role_F2(p, smem, q >> 1, q & 1, gr, w, l);
    }
    ENDSTAMP();
}

extern "C" void kernel_launch(void* const* d_in, const int* in_sizes, int n_in, void* d_out, int out_size, void* d_ws, size_t ws_size, hipStream_t stream) {
    Params p;
    p.x = (const unsigned short*)d_in[0]; p.mem0 = (const unsigned short*)d_in[1]; p.Wq = (const unsigned short*)d_in[2]; p.Wk = (const unsigned short*)d_in[3];
    p.Wv = (const unsigned short*)d_in[4]; p.Wo = (const unsigned short*)d_in[5]; p.w1 = (const unsigned short*)d_in[8]; p.b1 = (const unsigned short*)d_in[9];
    p.w2 = (const unsigned short*)d_in[10]; p.b2 = (const unsigned short*)d_in[11]; p.out = (unsigned short*)d_out; p.ws = (char*)d_ws;
    p.par = 0; p.dS = 3930; p.dM = 650; p.dP = 2530; p.fast = 150; p.slow = 1; p.up = 30; p.hold = 300;
    constexpr int LDS0 = 128 * 1024, LDS1 = 132 * 1024;
    (void)hipFuncSetAttribute((const void*)k0, hipFuncAttributeMaxDynamicSharedMemorySize, LDS0);
    (void)hipFuncSetAttribute((const void*)k1, hipFuncAttributeMaxDynamicSharedMemorySize, LDS1);
    hipLaunchKernelGGL(k0, dim3(256), dim3(256), LDS0, stream, p);
    hipLaunchKernelGGL(k1, dim3(256), dim3(256), LDS1, stream, p);
}
```
